```python
import torch

def _softmax(x, axis=-1):
    return torch.softmax(x, dim=axis)
def _gelu(x):
    return torch.nn.functional.gelu(x, approximate="tanh")
def _silu(x):
    return torch.nn.functional.silu(x)
def _sigmoid(x):
    return torch.sigmoid(x)
def _layernorm(x, axis=-1, eps=1e-5):
    m = x.mean(dim=axis, keepdim=True)
    v = x.var(dim=axis, keepdim=True, unbiased=False)
    return (x - m) / torch.sqrt(v + eps)
def _scatter_add(tgt, idx, src, axis=0):
    return tgt.clone().index_add_(axis, idx, src)
def _segment_sum(src, seg, n):
    out = torch.zeros((n,) + src.shape[1:], dtype=src.dtype)
    return out.index_add_(0, seg, src)
def _pad(x, pw):
    flat = tuple(v for pair in reversed(pw) for v in pair)
    return torch.nn.functional.pad(x, flat)

kernel_name = 'parallel_sum__ssm_conv_s5__mixer_channel_mlp__prompt'

def setup_inputs():

    _w = (lambda a: (lambda t: t.to(torch.bfloat16) if t.is_floating_point() else t)(torch.as_tensor(a)))
    def _A_setup():
        B, S, D = 1, 2048, 640
        P = D
        Lambda_re = -0.5 * (lambda _x: torch.exp(torch.as_tensor(_x)))((lambda *s: torch.randn(*s, dtype=torch.bfloat16) * 0.2)(P))
        Lambda_im = (lambda *s: torch.randn(*s, dtype=torch.bfloat16) * 0.8)(P) * 1.0
        B_re = (lambda *s: torch.randn(*s, dtype=torch.bfloat16) * 0.2)(P, D) * (D ** -0.5)
        B_im = (lambda *s: torch.randn(*s, dtype=torch.bfloat16) * 0.2)(P, D) * (D ** -0.5)
        C_re = (lambda *s: torch.randn(*s, dtype=torch.bfloat16) * 0.2)(D, P) * (P ** -0.5)
        C_im = (lambda *s: torch.randn(*s, dtype=torch.bfloat16) * 0.2)(D, P) * (P ** -0.5)
        log_dt = (lambda *s: torch.randn(*s, dtype=torch.bfloat16) * 0.2)(P) * 0.3 - 2.0
        Dskip = (lambda *s: torch.randn(*s, dtype=torch.bfloat16) * 0.05)(D)
        return {
            "x": _w((lambda *s: torch.randn(*s, dtype=torch.bfloat16) * 0.05)(B, S, D)),
            "Lambda_re": _w(Lambda_re),
            "Lambda_im": _w(Lambda_im),
            "B_re": _w(B_re),
            "B_im": _w(B_im),
            "C_re": _w(C_re),
            "C_im": _w(C_im),
            "log_dt": _w(log_dt),
            "Dskip": _w(Dskip),
        }
    _A_in = _A_setup()

    def _B_setup():
        B, S, D, F = 1, 2048, 640, 1536
        return {
            "x": _w((lambda *s: torch.randn(*s, dtype=torch.bfloat16) * 0.05)(B, S, D)),
            "w1": _w((lambda *s: torch.randn(*s, dtype=torch.bfloat16) * 1.6)(D, F) * (D ** -0.5)),
            "b1": _w((lambda *s: torch.randn(*s, dtype=torch.bfloat16) * 0.8)(F)),
            "w2": _w((lambda *s: torch.randn(*s, dtype=torch.bfloat16) * 0.05)(F, D) * (F ** -0.5)),
            "b2": _w((lambda *s: torch.randn(*s, dtype=torch.bfloat16) * 0.05)(D)),
        }
    _B_in = _B_setup()

    return {
        "A_x": _A_in["x"],
        "A_Lambda_re": _A_in["Lambda_re"],
        "A_Lambda_im": _A_in["Lambda_im"],
        "A_B_re": _A_in["B_re"],
        "A_B_im": _A_in["B_im"],
        "A_C_re": _A_in["C_re"],
        "A_C_im": _A_in["C_im"],
        "A_log_dt": _A_in["log_dt"],
        "A_Dskip": _A_in["Dskip"],
        "B_x": _B_in["x"],
        "B_w1": _B_in["w1"],
        "B_b1": _B_in["b1"],
        "B_w2": _B_in["w2"],
        "B_b2": _B_in["b2"],
    }

def reference(A_x, A_Lambda_re, A_Lambda_im, A_B_re, A_B_im, A_C_re, A_C_im, A_log_dt, A_Dskip, B_x, B_w1, B_b1, B_w2, B_b2):
    def _A_ref(x, Lambda_re, Lambda_im, B_re, B_im, C_re, C_im, log_dt, Dskip):
        Bsz, L, H = x.shape
        Lam = Lambda_re + 1j * Lambda_im
        Bmat = B_re + 1j * B_im
        Cmat = C_re + 1j * C_im
        dt = torch.exp(log_dt)
        Lam_bar = torch.exp(dt * Lam)
        B_bar = ((Lam_bar - 1.0) / Lam)[:, None] * Bmat
        Bu = torch.einsum("bld,pd->blp", x + 0j, B_bar)
        h = torch.zeros_like(Bu[:, 0, :])
        hs = []
        for t in range(L):
            h = Lam_bar[None, :] * h + Bu[:, t, :]
            hs.append(h)
        hseq = torch.stack(hs, 1)
        y = 2.0 * torch.einsum("blp,dp->bld", hseq, Cmat).real + x * Dskip[None, None, :]
        return y

    def _B_ref(x, w1, b1, w2, b2):
        y = _layernorm(x, axis=-1)
        h = _gelu(torch.einsum("bsd,df->bsf", y, w1) + b1)
        return torch.einsum("bsf,fd->bsd", h, w2) + b2

    x = A_x
    xn = _layernorm(x, axis=-1)
    return x + _A_ref(xn, A_Lambda_re, A_Lambda_im, A_B_re, A_B_im, A_C_re, A_C_im, A_log_dt, A_Dskip) + _B_ref(xn, B_w1, B_b1, B_w2, B_b2)
```

```cpp
#include <hip/hip_runtime.h>
#include <hip/hip_bf16.h>
#include <stdint.h>

#define L 2048
#define D 640
#define P 640
#define F 1536

typedef unsigned short u16;
__device__ __forceinline__ float bf2f(u16 v){ return __uint_as_float(((unsigned)v)<<16); }

__global__ void k_ln(const u16* x, float* xn, float* y){
  int t = blockIdx.x; int tid = threadIdx.x;
  float v[10]; float s=0;
  for(int i=0;i<10;i++){ v[i]=bf2f(x[t*D+tid+64*i]); s+=v[i]; }
  for(int o=32;o;o>>=1) s+=__shfl_xor(s,o);
  float m=s/D; float q=0;
  for(int i=0;i<10;i++){ v[i]-=m; q+=v[i]*v[i]; }
  for(int o=32;o;o>>=1) q+=__shfl_xor(q,o);
  float var=q/D; float r=rsqrtf(var+1e-5f);
  float var2=var*r*r; float r2=rsqrtf(var2+1e-5f);
  for(int i=0;i<10;i++){ xn[t*D+tid+64*i]=v[i]*r; y[t*D+tid+64*i]=v[i]*r*r2; }
}
__global__ void k_g1(const float* xn, const float* y, const u16* Bre, const u16* Bim, const u16* w1, const u16* b1, float* U, float* H){
  int t = blockIdx.y; int n = blockIdx.x*256+threadIdx.x;
  __shared__ float sx[D], sy[D];
  for(int i=threadIdx.x;i<D;i+=256){ sx[i]=xn[t*D+i]; sy[i]=y[t*D+i]; }
  __syncthreads();
  if(n<1280){
    const u16* w = (n<640? Bre+n*D : Bim+(n-640)*D);
    float a=0; for(int d=0;d<D;d++) a+=sx[d]*bf2f(w[d]);
    U[t*1280+n]=a;
  } else if(n<2816){
    int f=n-1280; float a=0; for(int d=0;d<D;d++) a+=sy[d]*bf2f(w1[d*F+f]);
    a+=bf2f(b1[f]);
    float u=0.7978845608028654f*(a+0.044715f*a*a*a);
    H[t*2816+1280+f]=0.5f*a*(1.f+tanhf(u));
  }
}
__global__ void k_scan(const float* U, const u16* Lre, const u16* Lim, const u16* ldt, float* H){
  int p = blockIdx.x*64+threadIdx.x;
  float lr=bf2f(Lre[p]), li=bf2f(Lim[p]);
  float dt=__bfloat162float(__float2bfloat16(expf(bf2f(ldt[p]))));
  float e=expf(dt*lr); float ar=e*cosf(dt*li), ai=e*sinf(dt*li);
  float nr=ar-1.f, ni=ai; float den=lr*lr+li*li;
  float cr=(nr*lr+ni*li)/den, ci=(ni*lr-nr*li)/den;
  float hr=0,hi=0;
  for(int t=0;t<L;t++){
    float ur=U[t*1280+p], ui=U[t*1280+640+p];
    float br=cr*ur-ci*ui, bi=cr*ui+ci*ur;
    float nhr=ar*hr-ai*hi+br, nhi=ar*hi+ai*hr+bi;
    hr=nhr; hi=nhi;
    H[t*2816+p]=2.f*hr; H[t*2816+640+p]=-2.f*hi;
  }
}
__global__ void k_g2(const float* H, const u16* x, const float* xn, const u16* Cre, const u16* Cim, const u16* w2, const u16* b2, const u16* Dsk, float* out){
  int t=blockIdx.x; __shared__ float sh[2816];
  for(int i=threadIdx.x;i<2816;i+=320) sh[i]=H[t*2816+i];
  __syncthreads();
  for(int d=threadIdx.x;d<D;d+=320){
    float a=0;
    for(int p=0;p<P;p++) a+=sh[p]*bf2f(Cre[d*P+p])+sh[640+p]*bf2f(Cim[d*P+p]);
    for(int f=0;f<F;f++) a+=sh[1280+f]*bf2f(w2[f*D+d]);
    out[t*D+d]=a+bf2f(b2[d])+bf2f(x[t*D+d])+xn[t*D+d]*bf2f(Dsk[d]);
  }
}

extern "C" void kernel_launch(void* const* d_in, const int* in_sizes, int n_in,
                              void* d_out, int out_size, void* d_ws, size_t ws_size,
                              hipStream_t stream) {
  const u16* x=(const u16*)d_in[0]; const u16* Lre=(const u16*)d_in[1]; const u16* Lim=(const u16*)d_in[2];
  const u16* Bre=(const u16*)d_in[3]; const u16* Bim=(const u16*)d_in[4]; const u16* Cre=(const u16*)d_in[5]; const u16* Cim=(const u16*)d_in[6];
  const u16* ldt=(const u16*)d_in[7]; const u16* Dsk=(const u16*)d_in[8];
  const u16* w1=(const u16*)d_in[10]; const u16* b1=(const u16*)d_in[11]; const u16* w2=(const u16*)d_in[12]; const u16* b2=(const u16*)d_in[13];
  float* ws=(float*)d_ws;
  float* xn=ws; float* y=xn+L*D; float* U=y+L*D; float* H=U+L*1280;
  k_ln<<<L,64,0,stream>>>(x,xn,y);
  k_g1<<<dim3(11,L),256,0,stream>>>(xn,y,Bre,Bim,w1,b1,U,H);
  k_scan<<<10,64,0,stream>>>(U,Lre,Lim,ldt,H);
  k_g2<<<L,320,0,stream>>>(H,x,xn,Cre,Cim,w2,b2,Dsk,(float*)d_out);
}
```

```cpp
#define AMP 0
#include <hip/hip_runtime.h>
#include <hip/hip_bf16.h>
#include <stdint.h>

#define L 2048
#define D 640
#define P 640
#define F 1536

typedef unsigned short u16;
__device__ __forceinline__ float bf2f(u16 v){ return __uint_as_float(((unsigned)v)<<16); }

__global__ void k_ln(const u16* x, float* xn, float* y){
  int t = blockIdx.x; int tid = threadIdx.x;
  float v[10]; float s=0;
  for(int i=0;i<10;i++){ v[i]=bf2f(x[t*D+tid+64*i]); s+=v[i]; }
  for(int o=32;o;o>>=1) s+=__shfl_xor(s,o);
  float m=s/D; float q=0;
  for(int i=0;i<10;i++){ v[i]-=m; q+=v[i]*v[i]; }
  for(int o=32;o;o>>=1) q+=__shfl_xor(q,o);
  float var=q/D; float r=rsqrtf(var+1e-5f);
  float var2=var*r*r; float r2=rsqrtf(var2+1e-5f);
  for(int i=0;i<10;i++){ xn[t*D+tid+64*i]=v[i]*r; y[t*D+tid+64*i]=v[i]*r*r2; }
}
__global__ void k_g1(const float* xn, const float* y, const u16* Bre, const u16* Bim, const u16* w1, const u16* b1, float* U, float* H){
  int t = blockIdx.y; int n = blockIdx.x*256+threadIdx.x;
  __shared__ float sx[D], sy[D];
  for(int i=threadIdx.x;i<D;i+=256){ sx[i]=xn[t*D+i]; sy[i]=y[t*D+i]; }
  __syncthreads();
  if(n<1280){
    const u16* w = (n<640? Bre+n*D : Bim+(n-640)*D);
    float a=0; for(int d=0;d<D;d++) a+=sx[d]*bf2f(w[d]);
    U[t*1280+n]=a;
  } else if(n<2816){
    int f=n-1280; float a=0; for(int d=0;d<D;d++) a+=sy[d]*bf2f(w1[d*F+f]);
    a+=bf2f(b1[f]);
    float u=0.7978845608028654f*(a+0.044715f*a*a*a);
    H[t*2816+1280+f]=0.5f*a*(1.f+tanhf(u));
  }
}
__global__ void k_scan(const float* U, const u16* Lre, const u16* Lim, const u16* ldt, float* H){
  int p = blockIdx.x*64+threadIdx.x;
  float lr=bf2f(Lre[p]), li=bf2f(Lim[p]);
  float dt=__bfloat162float(__float2bfloat16(expf(bf2f(ldt[p]))));
  float e=expf(dt*lr); float ar=e*cosf(dt*li), ai=e*sinf(dt*li);
  float nr=ar-1.f, ni=ai; float den=lr*lr+li*li;
  float cr=(nr*lr+ni*li)/den, ci=(ni*lr-nr*li)/den;
  float hr=0,hi=0;
  for(int t=0;t<L;t++){
    float ur=U[t*1280+p], ui=U[t*1280+640+p];
    float br=cr*ur-ci*ui, bi=cr*ui+ci*ur;
    float nhr=ar*hr-ai*hi+br, nhi=ar*hi+ai*hr+bi;
    hr=nhr; hi=nhi;
    H[t*2816+p]=2.f*hr; H[t*2816+640+p]=-2.f*hi;
  }
}
__global__ void k_g2(const float* H, const u16* x, const float* xn, const u16* Cre, const u16* Cim, const u16* w2, const u16* b2, const u16* Dsk, float* out){
  int t=blockIdx.x; __shared__ float sh[2816];
  for(int i=threadIdx.x;i<2816;i+=320) sh[i]=H[t*2816+i];
  __syncthreads();
  for(int d=threadIdx.x;d<D;d+=320){
    float a=0;
    for(int p=0;p<P;p++) a+=sh[p]*bf2f(Cre[d*P+p])+sh[640+p]*bf2f(Cim[d*P+p]);
    for(int f=0;f<F;f++) a+=sh[1280+f]*bf2f(w2[f*D+d]);
    out[t*D+d]=a+bf2f(b2[d])+bf2f(x[t*D+d])+xn[t*D+d]*bf2f(Dsk[d]);
  }
}


typedef unsigned int u32x4 __attribute__((ext_vector_type(4)));
struct Ptrs { const char* p[7]; };
__global__ __launch_bounds__(256,1) void k_probe(Ptrs P_, unsigned* dur){
  int b=blockIdx.x; int tid=threadIdx.x;
  unsigned long long t0=wall_clock64();
  u32x4 v[11]; u32x4 z={0,0,0,0};
  const char* x=P_.p[0]+b*10240;
  v[0]=*(const u32x4*)(x+tid*16); v[1]=*(const u32x4*)(x+4096+tid*16); v[2]= tid<128? *(const u32x4*)(x+8192+tid*16):z;
  #pragma unroll
  for(int i=0;i<4;i++) v[3+i]= tid<200? *(const u32x4*)(P_.p[1+i]+b*3200+tid*16):z;
  #pragma unroll
  for(int i=0;i<2;i++){ v[7+2*i]=*(const u32x4*)(P_.p[5+i]+b*7680+tid*16); v[8+2*i]= tid<224? *(const u32x4*)(P_.p[5+i]+b*7680+4096+tid*16):z; }
  u32x4 a=z;
  #pragma unroll
  for(int i=0;i<11;i++) a^=v[i];
  unsigned r=a.x^a.y^a.z^a.w;
  asm volatile(""::"v"(r));
  __syncthreads();
  unsigned long long t1=wall_clock64();
  if(tid==0) dur[b]=(unsigned)(t1-t0);
}
__global__ void k_spin(const unsigned* dur){
  unsigned s=0; for(int i=0;i<256;i++) s+=dur[i];
  unsigned long long n=(unsigned long long)s*AMP/256;
  unsigned long long t0=wall_clock64();
  while(wall_clock64()-t0<n) __builtin_amdgcn_s_sleep(8);
}

extern "C" void kernel_launch(void* const* d_in, const int* in_sizes, int n_in,
                              void* d_out, int out_size, void* d_ws, size_t ws_size,
                              hipStream_t stream) {
  const u16* x=(const u16*)d_in[0]; const u16* Lre=(const u16*)d_in[1]; const u16* Lim=(const u16*)d_in[2];
  const u16* Bre=(const u16*)d_in[3]; const u16* Bim=(const u16*)d_in[4]; const u16* Cre=(const u16*)d_in[5]; const u16* Cim=(const u16*)d_in[6];
  const u16* ldt=(const u16*)d_in[7]; const u16* Dsk=(const u16*)d_in[8];
  const u16* w1=(const u16*)d_in[10]; const u16* b1=(const u16*)d_in[11]; const u16* w2=(const u16*)d_in[12]; const u16* b2=(const u16*)d_in[13];
  float* ws=(float*)d_ws;
  float* xn=ws; float* y=xn+L*D; float* U=y+L*D; float* H=U+L*1280;
  Ptrs pp; pp.p[0]=(const char*)x; pp.p[1]=(const char*)Bre; pp.p[2]=(const char*)Bim; pp.p[3]=(const char*)Cre; pp.p[4]=(const char*)Cim; pp.p[5]=(const char*)w1; pp.p[6]=(const char*)w2;
  unsigned* dur=(unsigned*)(H+L*2816);
  k_probe<<<256,256,0,stream>>>(pp,dur);
  k_ln<<<L,64,0,stream>>>(x,xn,y);
  k_g1<<<dim3(11,L),256,0,stream>>>(xn,y,Bre,Bim,w1,b1,U,H);
  k_scan<<<10,64,0,stream>>>(U,Lre,Lim,ldt,H);
  k_g2<<<L,320,0,stream>>>(H,x,xn,Cre,Cim,w2,b2,Dsk,(float*)d_out);
  k_spin<<<1,1,0,stream>>>(dur);
}
```

```cpp
#include <hip/hip_runtime.h>
#include <stdint.h>

struct Srd { const void* p; uint32_t bytes; uint32_t fmt; };
struct Args { Srd d[15]; };

__global__ __launch_bounds__(512, 2) void s5_mlp_fused(Args a) {
#if __HIP_DEVICE_COMPILE__
  __shared__ __attribute__((aligned(16))) char smem[163840];
  auto kp = __builtin_amdgcn_kernarg_segment_ptr();
  unsigned bid = blockIdx.x, tid = threadIdx.x;
  unsigned lds0 = (unsigned)(size_t)(__attribute__((address_space(3))) char*)smem;
  asm volatile(
    "Lk_begin:\n"
    "s_getpc_b64 s[80:81]\n"
    "s_mov_b64 s[4:5], %0\n"
    "s_mov_b32 s6, %1\n"
    "s_load_dwordx16 s[8:23], s[4:5], 0\n"
    "s_load_dwordx16 s[24:39], s[4:5], 64\n"
    "s_load_dwordx16 s[40:55], s[4:5], 128\n"
    "s_load_dwordx8 s[56:63], s[4:5], 192\n"
    "s_load_dwordx4 s[64:67], s[4:5], 224\n"
    "v_lshrrev_b32 v5, 6, v0\n"
    "v_and_b32 v0, 63, v0\n"
    "v_readfirstlane_b32 s7, v5\n"
    "v_and_b32 v1, 15, v0\n"
    "v_lshrrev_b32 v2, 4, v0\n"
    "v_lshlrev_b32 v3, 4, v0\n"
    "s_and_b32 s92, s6, 7\n"
    "s_lshr_b32 s93, s6, 3\n"
    "s_lshr_b32 s69, s92, 1\n"
    "s_lshl_b32 s69, s69, 2\n"
    "s_lshr_b32 s94, s93, 3\n"
    "s_add_u32 s69, s69, s94\n"
    "s_and_b32 s93, s93, 7\n"
    "s_and_b32 s92, s92, 1\n"
    "s_lshl_b32 s70, s92, 3\n"
    "s_add_u32 s70, s70, s93\n"
    "s_lshr_b32 s79, s70, 3\n"
    "s_and_b32 s72, s70, 7\n"
    "s_lshl_b32 s71, s69, 1\n"
    "s_add_u32 s71, s71, s79\n"
    "s_and_b32 s68, s7, 3\n"
    "s_and_b32 s92, s6, 7\n"
    "s_mul_i32 s92, s92, 10\n"
    "s_lshr_b32 s92, s92, 3\n"
    "s_lshl_b32 s74, s92, 7\n"
    "s_mov_b32 s1, 0xffff0000\n"
    "s_cmp_ge_u32 s7, 4\n"
    "s_cbranch_scc1 Lk_roleM_1\n"
    "v_lshrrev_b32 v29, 3, v0\n"
    "v_and_b32 v30, 7, v0\n"
    "v_mul_u32_u24 v29, 1280, v29\n"
    "v_and_b32 v31, 1, v2\n"
    "v_xor_b32 v31, v31, v30\n"
    "v_lshl_add_u32 v5, v31, 4, v29\n"
    "s_mul_i32 s92, s68, 40960\n"
    "v_add_u32 v5, s92, v5\n"
    "v_xor_b32 v6, 32, v5\n"
    "v_add_u32 v6, 10240, v6\n"
    "v_xor_b32 v7, 64, v5\n"
    "v_add_u32 v7, 20480, v7\n"
    "v_xor_b32 v8, 96, v5\n"
    "v_add_u32 v8, 30720, v8\n"
    "v_lshlrev_b32 v31, 1, v2\n"
    "v_xor_b32 v31, v31, v30\n"
    "v_lshl_add_u32 v9, v31, 4, v29\n"
    "v_xor_b32 v10, 16, v9\n"
    "s_lshl_b32 s92, s68, 4\n"
    "v_xor_b32 v11, s92, v9\n"
    "s_mul_i32 s93, s68, 49152\n"
    "v_add_u32 v29, 0, v0\n"
    "v_mul_u32_u24 v30, 5462, v29\n"
    "v_lshrrev_b32 v30, 16, v30\n"
    "v_mul_u32_u24 v31, 12, v30\n"
    "v_sub_u32 v29, v29, v31\n"
    "v_bfe_u32 v31, v30, 3, 1\n"
    "v_lshlrev_b32 v31, 1, v31\n"
    "v_xor_b32 v29, v29, v31\n"
    "v_mul_u32_u24 v30, 3072, v30\n"
    "v_lshl_add_u32 v29, v29, 4, v30\n"
    "v_add_u32 v12, s93, v29\n"
    "v_add_u32 v29, 64, v0\n"
    "v_mul_u32_u24 v30, 5462, v29\n"
    "v_lshrrev_b32 v30, 16, v30\n"
    "v_mul_u32_u24 v31, 12, v30\n"
    "v_sub_u32 v29, v29, v31\n"
    "v_bfe_u32 v31, v30, 3, 1\n"
    "v_lshlrev_b32 v31, 1, v31\n"
    "v_xor_b32 v29, v29, v31\n"
    "v_mul_u32_u24 v30, 3072, v30\n"
    "v_lshl_add_u32 v29, v29, 4, v30\n"
    "v_add_u32 v13, s93, v29\n"
    "v_add_u32 v29, 128, v0\n"
    "v_mul_u32_u24 v30, 5462, v29\n"
    "v_lshrrev_b32 v30, 16, v30\n"
    "v_mul_u32_u24 v31, 12, v30\n"
    "v_sub_u32 v29, v29, v31\n"
    "v_bfe_u32 v31, v30, 3, 1\n"
    "v_lshlrev_b32 v31, 1, v31\n"
    "v_xor_b32 v29, v29, v31\n"
    "v_mul_u32_u24 v30, 3072, v30\n"
    "v_lshl_add_u32 v29, v29, 4, v30\n"
    "v_add_u32 v14, s93, v29\n"
    "s_mul_i32 s4, s69, 163840\n"
    "s_mul_i32 s5, s70, 51200\n"
    "s_add_u32 s3, s5, 40960\n"
    "s_mul_i32 s92, s68, 10240\n"
    "s_add_u32 s5, s5, s92\n"
    "s_mul_i32 s2, s70, 192\n"
    "s_lshl_b32 s84, s68, 12\n"
    "s_lshl_b32 s85, s68, 10\n"
    "s_waitcnt lgkmcnt(0)\n"
    "s_mul_i32 s50, s68, 3072\n"
    "s_mul_i32 s51, s68, 5120\n"
    "s_waitcnt lgkmcnt(0)\n"
    "s_load_dword s73, s[24:25], 0\n"
    "s_cmp_eq_u32 s68, 0\n"
    "s_cselect_b64 s[80:81], s[12:13], s[16:17]\n"
    "s_mov_b64 s[82:83], s[14:15]\n"
    "s_add_u32 s75, s4, s74\n"
    "s_add_u32 s76, s5, s74\n"
    "s_add_u32 s78, s3, s74\n"
    "s_mul_i32 s77, s74, 1536\n"
    "s_add_u32 s77, s77, s2\n"
    "s_add_u32 s74, s74, 128\n"
    "s_cmp_eq_u32 s74, 1280\n"
    "s_cselect_b32 s74, 0, s74\n"
    "s_add_u32 m0, s84, 0\n"
    "s_nop 0\n"
    "buffer_load_dwordx4 v5, s[8:11], s75 offen lds\n"
    "s_add_u32 m0, s84, 1024\n"
    "s_nop 0\n"
    "buffer_load_dwordx4 v6, s[8:11], s75 offen lds\n"
    "s_add_u32 m0, s84, 2048\n"
    "s_nop 0\n"
    "buffer_load_dwordx4 v7, s[8:11], s75 offen lds\n"
    "s_add_u32 m0, s84, 3072\n"
    "s_nop 0\n"
    "buffer_load_dwordx4 v8, s[8:11], s75 offen lds\n"
    "s_add_u32 m0, s85, 16384\n"
    "s_nop 0\n"
    "buffer_load_dwordx4 v9, s[12:15], s76 offen lds\n"
    "s_add_u32 m0, s85, 21504\n"
    "s_nop 0\n"
    "buffer_load_dwordx4 v10, s[16:19], s76 offen lds\n"
    "s_cmp_lt_u32 s68, 2\n"
    "s_cbranch_scc0 Lk_skip_2\n"
    "s_add_u32 m0, s51, 20480\n"
    "s_nop 0\n"
    "buffer_load_dwordx4 v11, s[80:83], s78 offen lds\n"
    "Lk_skip_2:\n"
    "s_add_u32 m0, s50, 26624\n"
    "s_nop 0\n"
    "buffer_load_dwordx4 v12, s[20:23], s77 offen lds\n"
    "s_add_u32 m0, s50, 27648\n"
    "s_nop 0\n"
    "buffer_load_dwordx4 v13, s[20:23], s77 offen lds\n"
    "s_add_u32 m0, s50, 28672\n"
    "s_nop 0\n"
    "buffer_load_dwordx4 v14, s[20:23], s77 offen lds\n"
    "v_lshrrev_b32 v30, 2, v1\n"
    "v_lshlrev_b32 v31, 1, v30\n"
    "v_bfe_u32 v29, v1, 1, 1\n"
    "v_or_b32 v31, v31, v29\n"
    "v_xor_b32 v31, v31, v2\n"
    "v_lshlrev_b32 v31, 4, v31\n"
    "v_lshlrev_b32 v30, 3, v30\n"
    "v_and_b32 v29, 3, v1\n"
    "v_add_u32 v30, v30, v29\n"
    "v_lshl_add_u32 v15, v30, 7, v31\n"
    "s_lshl_b32 s92, s68, 12\n"
    "v_add_u32 v15, s92, v15\n"
    "v_xor_b32 v16, 64, v15\n"
    "v_add_u32 v17, 77824, v15\n"
    "v_add_u32 v18, 77824, v16\n"
    "v_and_b32 v29, 1, v1\n"
    "v_lshrrev_b32 v30, 1, v1\n"
    "v_and_b32 v31, 6, v30\n"
    "v_or_b32 v31, v31, v29\n"
    "v_xor_b32 v31, v31, v2\n"
    "v_lshlrev_b32 v31, 4, v31\n"
    "v_mad_u32_u24 v29, v29, 40, v30\n"
    "v_lshl_add_u32 v29, v29, 7, v31\n"
    "v_add_u32 v19, 16384, v29\n"
    "v_xor_b32 v20, 64, v19\n"
    "v_add_u32 v21, 77824, v19\n"
    "v_add_u32 v22, 77824, v20\n"
    "s_mul_i32 s95, s79, 24\n"
    "s_mul_i32 s92, s68, 2\n"
    "s_add_u32 s95, s95, s92\n"
    "s_mul_i32 s92, s72, 88\n"
    "s_add_u32 s92, s92, s95\n"
    "s_mul_i32 s92, s92, 5120\n"
    "s_add_u32 s52, s92, 344064\n"
    "s_add_u32 s53, s92, 349184\n"
    "s_xor_b32 s92, s71, 1\n"
    "s_mul_i32 s92, s92, 88\n"
    "s_add_u32 s92, s92, s95\n"
    "s_lshl_b32 s92, s92, 12\n"
    "s_add_u32 s40, s92, 4259840\n"
    "s_add_u32 s41, s92, 4263936\n"
    "s_xor_b32 s92, s71, 0\n"
    "s_mul_i32 s92, s92, 88\n"
    "s_add_u32 s92, s92, s95\n"
    "s_lshl_b32 s92, s92, 12\n"
    "s_add_u32 s45, s92, 4259840\n"
    "s_add_u32 s46, s92, 4263936\n"
    "s_mul_i32 s95, s79, 20\n"
    "s_mul_i32 s92, s68, 3\n"
    "s_add_u32 s95, s95, s92\n"
    "s_mul_i32 s92, s72, 88\n"
    "s_add_u32 s92, s92, s95\n"
    "s_mul_i32 s92, s92, 5120\n"
    "s_add_u32 s54, s92, 548864\n"
    "s_add_u32 s55, s92, 553984\n"
    "s_add_u32 s56, s92, 559104\n"
    "s_xor_b32 s92, s71, 1\n"
    "s_mul_i32 s92, s92, 88\n"
    "s_add_u32 s92, s92, s95\n"
    "s_lshl_b32 s92, s92, 12\n"
    "s_add_u32 s75, s4, s74\n"
    "s_add_u32 s76, s5, s74\n"
    "s_add_u32 s78, s3, s74\n"
    "s_mul_i32 s77, s74, 1536\n"
    "s_add_u32 s77, s77, s2\n"
    "s_add_u32 s74, s74, 128\n"
    "s_cmp_eq_u32 s74, 1280\n"
    "s_cselect_b32 s74, 0, s74\n"
    "s_add_u32 m0, s84, 38912\n"
    "s_nop 0\n"
    "buffer_load_dwordx4 v5, s[8:11], s75 offen lds\n"
    "s_add_u32 s42, s92, 4423680\n"
    "s_add_u32 s43, s92, 4427776\n"
    "s_add_u32 s44, s92, 4431872\n"
    "s_xor_b32 s92, s71, 0\n"
    "s_mul_i32 s92, s92, 88\n"
    "s_add_u32 s92, s92, s95\n"
    "s_lshl_b32 s92, s92, 12\n"
    "s_add_u32 s47, s92, 4423680\n"
    "s_add_u32 s48, s92, 4427776\n"
    "s_add_u32 s49, s92, 4431872\n"
    "s_lshl_b32 s92, s69, 4\n"
    "s_add_u32 m0, s84, 39936\n"
    "s_nop 0\n"
    "buffer_load_dwordx4 v6, s[8:11], s75 offen lds\n"
    "s_add_u32 s93, s92, s70\n"
    "s_lshl_b32 s93, s93, 4\n"
    "s_add_u32 s93, s93, s68\n"
    "s_lshl_b32 s93, s93, 2\n"
    "s_add_u32 s98, s93, 24576\n"
    "s_lshl_b32 s93, s79, 3\n"
    "s_add_u32 s92, s92, s93\n"
    "s_lshl_b32 s95, s68, 2\n"
    "s_lshr_b32 s93, 26213, s95\n"
    "s_and_b32 s93, s93, 15\n"
    "s_add_u32 s93, s93, s92\n"
    "s_add_u32 m0, s84, 40960\n"
    "s_nop 0\n"
    "buffer_load_dwordx4 v7, s[8:11], s75 offen lds\n"
    "s_lshl_b32 s93, s93, 6\n"
    "s_add_u32 s99, s93, 8192\n"
    "s_lshr_b32 s93, 25923, s95\n"
    "s_and_b32 s93, s93, 15\n"
    "s_add_u32 s93, s93, s92\n"
    "s_lshl_b32 s93, s93, 6\n"
    "s_add_u32 s59, s93, 24576\n"
    "s_lshr_b32 s92, s68, 1\n"
    "s_lshl_b32 s93, s69, 1\n"
    "s_add_u32 s94, s92, s93\n"
    "s_mul_i32 s94, s94, 360448\n"
    "s_add_u32 m0, s84, 41984\n"
    "s_nop 0\n"
    "buffer_load_dwordx4 v8, s[8:11], s75 offen lds\n"
    "s_and_b32 s93, s68, 1\n"
    "s_lshl_b32 s93, s93, 11\n"
    "s_add_u32 s94, s94, s93\n"
    "s_add_u32 s94, s94, 4194304\n"
    "s_add_u32 s28, s94, 196608\n"
    "s_mul_i32 s30, s70, 640\n"
    "s_add_u32 s30, s30, 40960\n"
    "s_mul_i32 s29, s69, 10240\n"
    "s_add_u32 s29, s29, s30\n"
    "s_xor_b32 s92, s71, 0\n"
    "s_lshl_b32 s92, s92, 1\n"
    "s_add_u32 m0, s85, 55296\n"
    "s_nop 0\n"
    "buffer_load_dwordx4 v9, s[12:15], s76 offen lds\n"
    "s_lshr_b32 s93, s68, 1\n"
    "s_add_u32 s92, s92, s93\n"
    "s_lshl_b32 s92, s92, 3\n"
    "s_and_b32 s93, s68, 1\n"
    "s_add_u32 s92, s92, s93\n"
    "s_mul_i32 s92, s92, 10240\n"
    "s_mul_i32 s93, s72, 320\n"
    "s_add_u32 s31, s92, s93\n"
    "s_lshl_b32 s92, s71, 3\n"
    "s_add_u32 s92, s92, s72\n"
    "s_lshl_b32 s92, s92, 2\n"
    "s_add_u32 m0, s85, 60416\n"
    "s_nop 0\n"
    "buffer_load_dwordx4 v10, s[16:19], s76 offen lds\n"
    "s_add_u32 s92, s92, s68\n"
    "s_lshl_b32 s92, s92, 12\n"
    "s_add_u32 s58, s92, 16777216\n"
    "s_xor_b32 s57, s58, 131072\n"
    "s_mul_i32 s33, s68, 15360\n"
    "s_add_u32 s33, s33, 81920\n"
    "s_cmp_eq_u32 s68, 1\n"
    "s_cselect_b64 s[34:35], -1, 0\n"
    "s_cmp_eq_u32 s68, 2\n"
    "s_cselect_b64 s[36:37], -1, 0\n"
    "s_cmp_eq_u32 s68, 3\n"
    "s_cselect_b64 s[38:39], -1, 0\n"
    "s_mov_b32 s88, 0xffff0000\n"
    "s_mov_b32 s89, 0xffff0000\n"
    "s_cmp_lt_u32 s68, 2\n"
    "s_cbranch_scc0 Lk_skip_3\n"
    "s_add_u32 m0, s51, 59392\n"
    "s_nop 0\n"
    "buffer_load_dwordx4 v11, s[80:83], s78 offen lds\n"
    "Lk_skip_3:\n"
    "s_lshl_b32 s92, s68, 7\n"
    "s_add_u32 s92, s92, 158976\n"
    "v_lshl_add_u32 v35, v2, 4, s92\n"
    "v_lshlrev_b32 v23, 4, v1\n"
    "v_add_u32 v23, 133120, v23\n"
    "s_lshl_b32 s92, s68, 9\n"
    "v_lshl_add_u32 v34, v1, 4, s92\n"
    "v_mov_b32 v29, 2048\n"
    "v_cmp_gt_u32 vcc, 16, v0\n"
    "s_nop 1\n"
    "v_cndmask_b32 v34, v29, v34, vcc\n"
    "v_add_u32 v34, 135936, v34\n"
    "v_min_u32 v29, 9, v1\n"
    "s_add_u32 m0, s50, 65536\n"
    "s_nop 0\n"
    "buffer_load_dwordx4 v12, s[20:23], s77 offen lds\n"
    "s_mul_i32 s92, s68, 10\n"
    "s_add_u32 s92, s92, 0\n"
    "v_add_u32 v29, s92, v29\n"
    "v_lshlrev_b32 v171, 5, v29\n"
    "v_add_u32 v171, 133120, v171\n"
    "v_mul_u32_u24 v30, 1408, v2\n"
    "v_lshl_add_u32 v169, v29, 4, v30\n"
    "v_mov_b32 v30, 0x40000000\n"
    "v_cmp_gt_u32 vcc, 32, v0\n"
    "s_nop 1\n"
    "v_cndmask_b32 v170, v30, v169, vcc\n"
    "s_mul_i32 s60, s70, 14080\n"
    "s_add_u32 s60, s60, 3866624\n"
    "s_add_u32 m0, s50, 66560\n"
    "s_nop 0\n"
    "buffer_load_dwordx4 v13, s[20:23], s77 offen lds\n"
    "s_add_u32 s61, s60, 5632\n"
    "s_add_u32 s62, s60, 11264\n"
    "s_mov_b64 s[86:87], 0xffffffff\n"
    "v_lshrrev_b32 v29, 2, v1\n"
    "v_and_b32 v30, 3, v1\n"
    "v_lshl_add_u32 v29, v29, 3, v30\n"
    "v_and_b32 v30, 1, v2\n"
    "v_lshl_add_u32 v29, v30, 2, v29\n"
    "v_lshlrev_b32 v168, 4, v29\n"
    "s_lshl_b32 s63, s69, 2\n"
    "s_add_u32 s63, s63, s68\n"
    "s_add_u32 m0, s50, 67584\n"
    "s_nop 0\n"
    "buffer_load_dwordx4 v14, s[20:23], s77 offen lds\n"
    "s_lshl_b32 s63, s63, 9\n"
    "s_add_u32 s63, s63, 204800\n"
    "v_and_b32 v29, 31, v0\n"
    "v_lshlrev_b32 v29, 2, v29\n"
    "s_lshl_b32 s92, s68, 7\n"
    "s_add_u32 s92, s92, 158976\n"
    "v_add_u32 v172, s92, v29\n"
    "v_subrev_u32 v173, 158976, v172\n"
    "v_lshlrev_b32 v173, 2, v173\n"
    "v_add_u32 v173, 135936, v173\n"
    "s_waitcnt lgkmcnt(0)\n"
    "s_mul_i32 s73, s73, 0x9E3779B1\n"
    "s_add_u32 s73, s73, 0x7F4A7C15\n"
    "s_waitcnt vmcnt(9)\n"
    "s_barrier\n"
    "ds_read_b128 v[96:99], v15 offset:0\n"
    "ds_read_b128 v[112:115], v19 offset:0\n"
    "ds_read_b128 v[100:103], v15 offset:512\n"
    "ds_read_b128 v[116:119], v19 offset:1024\n"
    "ds_read_b128 v[120:123], v19 offset:2048\n"
    "ds_read_b128 v[124:127], v19 offset:3072\n"
    "ds_read_b128 v[128:131], v19 offset:4096\n"
    "s_waitcnt lgkmcnt(0)\n"
    "v_mfma_f32_16x16x32_bf16 v[36:39], v[96:99], v[112:115], 0\n"
    "s_add_u32 s75, s4, s74\n"
    "s_add_u32 s76, s5, s74\n"
    "s_add_u32 s78, s3, s74\n"
    "s_mul_i32 s77, s74, 1536\n"
    "s_add_u32 s77, s77, s2\n"
    "s_add_u32 s74, s74, 128\n"
    "s_cmp_eq_u32 s74, 1280\n"
    "s_cselect_b32 s74, 0, s74\n"
    "s_add_u32 m0, s84, 77824\n"
    "s_nop 0\n"
    "buffer_load_dwordx4 v5, s[8:11], s75 offen lds\n"
    "ds_read_b128 v[104:107], v16 offset:0\n"
    "s_add_u32 m0, s84, 78848\n"
    "s_nop 0\n"
    "buffer_load_dwordx4 v6, s[8:11], s75 offen lds\n"
    "ds_read_b128 v[132:135], v20 offset:0\n"
    "v_mfma_f32_16x16x32_bf16 v[56:59], v[100:103], v[112:115], 0\n"
    "s_add_u32 m0, s84, 79872\n"
    "s_nop 0\n"
    "buffer_load_dwordx4 v7, s[8:11], s75 offen lds\n"
    "ds_read_b128 v[108:111], v16 offset:512\n"
    "v_mfma_f32_16x16x32_bf16 v[40:43], v[96:99], v[116:119], 0\n"
    "s_add_u32 m0, s84, 80896\n"
    "s_nop 0\n"
    "buffer_load_dwordx4 v8, s[8:11], s75 offen lds\n"
    "ds_read_b128 v[136:139], v20 offset:1024\n"
    "s_add_u32 m0, s85, 94208\n"
    "s_nop 0\n"
    "buffer_load_dwordx4 v9, s[12:15], s76 offen lds\n"
    "v_mfma_f32_16x16x32_bf16 v[60:63], v[100:103], v[116:119], 0\n"
    "ds_read_b128 v[140:143], v20 offset:2048\n"
    "s_add_u32 m0, s85, 99328\n"
    "s_nop 0\n"
    "buffer_load_dwordx4 v10, s[16:19], s76 offen lds\n"
    "v_mfma_f32_16x16x32_bf16 v[44:47], v[96:99], v[120:123], 0\n"
    "ds_read_b128 v[144:147], v20 offset:3072\n"
    "s_cmp_lt_u32 s68, 2\n"
    "s_cbranch_scc0 Lk_skip_4\n"
    "s_add_u32 m0, s51, 98304\n"
    "s_nop 0\n"
    "buffer_load_dwordx4 v11, s[80:83], s78 offen lds\n"
    "Lk_skip_4:\n"
    "v_mfma_f32_16x16x32_bf16 v[64:67], v[100:103], v[120:123], 0\n"
    "s_add_u32 m0, s50, 104448\n"
    "s_nop 0\n"
    "buffer_load_dwordx4 v12, s[20:23], s77 offen lds\n"
    "ds_read_b128 v[148:151], v20 offset:4096\n"
    "v_mfma_f32_16x16x32_bf16 v[48:51], v[96:99], v[124:127], 0\n"
    "s_add_u32 m0, s50, 105472\n"
    "s_nop 0\n"
    "buffer_load_dwordx4 v13, s[20:23], s77 offen lds\n"
    "v_mfma_f32_16x16x32_bf16 v[68:71], v[100:103], v[124:127], 0\n"
    "v_mfma_f32_16x16x32_bf16 v[52:55], v[96:99], v[128:131], 0\n"
    "s_add_u32 m0, s50, 106496\n"
    "s_nop 0\n"
    "buffer_load_dwordx4 v14, s[20:23], s77 offen lds\n"
    "v_mfma_f32_16x16x32_bf16 v[72:75], v[100:103], v[128:131], 0\n"
    "s_waitcnt vmcnt(9) lgkmcnt(0)\n"
    "s_barrier\n"
    "v_mfma_f32_16x16x32_bf16 v[36:39], v[104:107], v[132:135], v[36:39]\n"
    "s_add_u32 s75, s4, s74\n"
    "s_add_u32 s76, s5, s74\n"
    "s_add_u32 s78, s3, s74\n"
    "s_mul_i32 s77, s74, 1536\n"
    "s_add_u32 s77, s77, s2\n"
    "s_add_u32 s74, s74, 128\n"
    "s_cmp_eq_u32 s74, 1280\n"
    "s_cselect_b32 s74, 0, s74\n"
    "s_add_u32 m0, s84, 0\n"
    "s_nop 0\n"
    "buffer_load_dwordx4 v5, s[8:11], s75 offen lds\n"
    "ds_read_b128 v[96:99], v15 offset:38912\n"
    "ds_read_b128 v[112:115], v19 offset:38912\n"
    "v_mfma_f32_16x16x32_bf16 v[56:59], v[108:111], v[132:135], v[56:59]\n"
    "s_add_u32 m0, s84, 1024\n"
    "s_nop 0\n"
    "buffer_load_dwordx4 v6, s[8:11], s75 offen lds\n"
    "ds_read_b128 v[100:103], v15 offset:39424\n"
    "v_mfma_f32_16x16x32_bf16 v[40:43], v[104:107], v[136:139], v[40:43]\n"
    "ds_read_b128 v[116:119], v19 offset:39936\n"
    "s_add_u32 m0, s84, 2048\n"
    "s_nop 0\n"
    "buffer_load_dwordx4 v7, s[8:11], s75 offen lds\n"
    "v_mfma_f32_16x16x32_bf16 v[60:63], v[108:111], v[136:139], v[60:63]\n"
    "ds_read_b128 v[120:123], v19 offset:40960\n"
    "v_mfma_f32_16x16x32_bf16 v[44:47], v[104:107], v[140:143], v[44:47]\n"
    "ds_read_b128 v[124:127], v19 offset:41984\n"
    "s_add_u32 m0, s84, 3072\n"
    "s_nop 0\n"
    "buffer_load_dwordx4 v8, s[8:11], s75 offen lds\n"
    "v_mfma_f32_16x16x32_bf16 v[64:67], v[108:111], v[140:143], v[64:67]\n"
    "ds_read_b128 v[128:131], v19 offset:43008\n"
    "v_mfma_f32_16x16x32_bf16 v[48:51], v[104:107], v[144:147], v[48:51]\n"
    "s_add_u32 m0, s85, 16384\n"
    "s_nop 0\n"
    "buffer_load_dwordx4 v9, s[12:15], s76 offen lds\n"
    "v_mfma_f32_16x16x32_bf16 v[68:71], v[108:111], v[144:147], v[68:71]\n"
    "v_mfma_f32_16x16x32_bf16 v[52:55], v[104:107], v[148:151], v[52:55]\n"
    "v_mfma_f32_16x16x32_bf16 v[72:75], v[108:111], v[148:151], v[72:75]\n"
    "s_waitcnt lgkmcnt(0)\n"
    "v_mfma_f32_16x16x32_bf16 v[36:39], v[96:99], v[112:115], v[36:39]\n"
    "s_add_u32 m0, s85, 21504\n"
    "s_nop 0\n"
    "buffer_load_dwordx4 v10, s[16:19], s76 offen lds\n"
    "ds_read_b128 v[104:107], v16 offset:38912\n"
    "ds_read_b128 v[132:135], v20 offset:38912\n"
    "v_mfma_f32_16x16x32_bf16 v[56:59], v[100:103], v[112:115], v[56:59]\n"
    "s_cmp_lt_u32 s68, 2\n"
    "s_cbranch_scc0 Lk_skip_5\n"
    "s_add_u32 m0, s51, 20480\n"
    "s_nop 0\n"
    "buffer_load_dwordx4 v11, s[80:83], s78 offen lds\n"
    "Lk_skip_5:\n"
    "ds_read_b128 v[108:111], v16 offset:39424\n"
    "v_mfma_f32_16x16x32_bf16 v[40:43], v[96:99], v[116:119], v[40:43]\n"
    "ds_read_b128 v[136:139], v20 offset:39936\n"
    "s_add_u32 m0, s50, 26624\n"
    "s_nop 0\n"
    "buffer_load_dwordx4 v12, s[20:23], s77 offen lds\n"
    "v_mfma_f32_16x16x32_bf16 v[60:63], v[100:103], v[116:119], v[60:63]\n"
    "ds_read_b128 v[140:143], v20 offset:40960\n"
    "v_mfma_f32_16x16x32_bf16 v[44:47], v[96:99], v[120:123], v[44:47]\n"
    "ds_read_b128 v[144:147], v20 offset:41984\n"
    "s_add_u32 m0, s50, 27648\n"
    "s_nop 0\n"
    "buffer_load_dwordx4 v13, s[20:23], s77 offen lds\n"
    "v_mfma_f32_16x16x32_bf16 v[64:67], v[100:103], v[120:123], v[64:67]\n"
    "ds_read_b128 v[148:151], v20 offset:43008\n"
    "v_mfma_f32_16x16x32_bf16 v[48:51], v[96:99], v[124:127], v[48:51]\n"
    "s_add_u32 m0, s50, 28672\n"
    "s_nop 0\n"
    "buffer_load_dwordx4 v14, s[20:23], s77 offen lds\n"
    "v_mfma_f32_16x16x32_bf16 v[68:71], v[100:103], v[124:127], v[68:71]\n"
    "v_mfma_f32_16x16x32_bf16 v[52:55], v[96:99], v[128:131], v[52:55]\n"
    "v_mfma_f32_16x16x32_bf16 v[72:75], v[100:103], v[128:131], v[72:75]\n"
    "s_waitcnt vmcnt(9) lgkmcnt(0)\n"
    "s_barrier\n"
    "v_mfma_f32_16x16x32_bf16 v[36:39], v[104:107], v[132:135], v[36:39]\n"
    "s_add_u32 s75, s4, s74\n"
    "s_add_u32 s76, s5, s74\n"
    "s_add_u32 s78, s3, s74\n"
    "s_mul_i32 s77, s74, 1536\n"
    "s_add_u32 s77, s77, s2\n"
    "s_add_u32 s74, s74, 128\n"
    "s_cmp_eq_u32 s74, 1280\n"
    "s_cselect_b32 s74, 0, s74\n"
    "s_add_u32 m0, s84, 38912\n"
    "s_nop 0\n"
    "buffer_load_dwordx4 v5, s[8:11], s75 offen lds\n"
    "ds_read_b128 v[96:99], v17 offset:0\n"
    "ds_read_b128 v[112:115], v21 offset:0\n"
    "v_mfma_f32_16x16x32_bf16 v[56:59], v[108:111], v[132:135], v[56:59]\n"
    "s_add_u32 m0, s84, 39936\n"
    "s_nop 0\n"
    "buffer_load_dwordx4 v6, s[8:11], s75 offen lds\n"
    "ds_read_b128 v[100:103], v17 offset:512\n"
    "v_mfma_f32_16x16x32_bf16 v[40:43], v[104:107], v[136:139], v[40:43]\n"
    "ds_read_b128 v[116:119], v21 offset:1024\n"
    "s_add_u32 m0, s84, 40960\n"
    "s_nop 0\n"
    "buffer_load_dwordx4 v7, s[8:11], s75 offen lds\n"
    "v_mfma_f32_16x16x32_bf16 v[60:63], v[108:111], v[136:139], v[60:63]\n"
    "ds_read_b128 v[120:123], v21 offset:2048\n"
    "v_mfma_f32_16x16x32_bf16 v[44:47], v[104:107], v[140:143], v[44:47]\n"
    "ds_read_b128 v[124:127], v21 offset:3072\n"
    "s_add_u32 m0, s84, 41984\n"
    "s_nop 0\n"
    "buffer_load_dwordx4 v8, s[8:11], s75 offen lds\n"
    "v_mfma_f32_16x16x32_bf16 v[64:67], v[108:111], v[140:143], v[64:67]\n"
    "ds_read_b128 v[128:131], v21 offset:4096\n"
    "v_mfma_f32_16x16x32_bf16 v[48:51], v[104:107], v[144:147], v[48:51]\n"
    "s_add_u32 m0, s85, 55296\n"
    "s_nop 0\n"
    "buffer_load_dwordx4 v9, s[12:15], s76 offen lds\n"
    "v_mfma_f32_16x16x32_bf16 v[68:71], v[108:111], v[144:147], v[68:71]\n"
    "v_mfma_f32_16x16x32_bf16 v[52:55], v[104:107], v[148:151], v[52:55]\n"
    "v_mfma_f32_16x16x32_bf16 v[72:75], v[108:111], v[148:151], v[72:75]\n"
    "s_waitcnt lgkmcnt(0)\n"
    "v_mfma_f32_16x16x32_bf16 v[36:39], v[96:99], v[112:115], v[36:39]\n"
    "s_add_u32 m0, s85, 60416\n"
    "s_nop 0\n"
    "buffer_load_dwordx4 v10, s[16:19], s76 offen lds\n"
    "ds_read_b128 v[104:107], v18 offset:0\n"
    "ds_read_b128 v[132:135], v22 offset:0\n"
    "v_mfma_f32_16x16x32_bf16 v[56:59], v[100:103], v[112:115], v[56:59]\n"
    "s_cmp_lt_u32 s68, 2\n"
    "s_cbranch_scc0 Lk_skip_6\n"
    "s_add_u32 m0, s51, 59392\n"
    "s_nop 0\n"
    "buffer_load_dwordx4 v11, s[80:83], s78 offen lds\n"
    "Lk_skip_6:\n"
    "ds_read_b128 v[108:111], v18 offset:512\n"
    "v_mfma_f32_16x16x32_bf16 v[40:43], v[96:99], v[116:119], v[40:43]\n"
    "ds_read_b128 v[136:139], v22 offset:1024\n"
    "s_add_u32 m0, s50, 65536\n"
    "s_nop 0\n"
    "buffer_load_dwordx4 v12, s[20:23], s77 offen lds\n"
    "v_mfma_f32_16x16x32_bf16 v[60:63], v[100:103], v[116:119], v[60:63]\n"
    "ds_read_b128 v[140:143], v22 offset:2048\n"
    "v_mfma_f32_16x16x32_bf16 v[44:47], v[96:99], v[120:123], v[44:47]\n"
    "ds_read_b128 v[144:147], v22 offset:3072\n"
    "s_add_u32 m0, s50, 66560\n"
    "s_nop 0\n"
    "buffer_load_dwordx4 v13, s[20:23], s77 offen lds\n"
    "v_mfma_f32_16x16x32_bf16 v[64:67], v[100:103], v[120:123], v[64:67]\n"
    "ds_read_b128 v[148:151], v22 offset:4096\n"
    "v_mfma_f32_16x16x32_bf16 v[48:51], v[96:99], v[124:127], v[48:51]\n"
    "s_add_u32 m0, s50, 67584\n"
    "s_nop 0\n"
    "buffer_load_dwordx4 v14, s[20:23], s77 offen lds\n"
    "v_mfma_f32_16x16x32_bf16 v[68:71], v[100:103], v[124:127], v[68:71]\n"
    "v_mfma_f32_16x16x32_bf16 v[52:55], v[96:99], v[128:131], v[52:55]\n"
    "v_mfma_f32_16x16x32_bf16 v[72:75], v[100:103], v[128:131], v[72:75]\n"
    "s_waitcnt vmcnt(9) lgkmcnt(0)\n"
    "s_barrier\n"
    "v_mfma_f32_16x16x32_bf16 v[36:39], v[104:107], v[132:135], v[36:39]\n"
    "s_add_u32 s75, s4, s74\n"
    "s_add_u32 s76, s5, s74\n"
    "s_add_u32 s78, s3, s74\n"
    "s_mul_i32 s77, s74, 1536\n"
    "s_add_u32 s77, s77, s2\n"
    "s_add_u32 s74, s74, 128\n"
    "s_cmp_eq_u32 s74, 1280\n"
    "s_cselect_b32 s74, 0, s74\n"
    "s_add_u32 m0, s84, 77824\n"
    "s_nop 0\n"
    "buffer_load_dwordx4 v5, s[8:11], s75 offen lds\n"
    "ds_read_b128 v[96:99], v15 offset:0\n"
    "ds_read_b128 v[112:115], v19 offset:0\n"
    "v_mfma_f32_16x16x32_bf16 v[56:59], v[108:111], v[132:135], v[56:59]\n"
    "s_add_u32 m0, s84, 78848\n"
    "s_nop 0\n"
    "buffer_load_dwordx4 v6, s[8:11], s75 offen lds\n"
    "ds_read_b128 v[100:103], v15 offset:512\n"
    "v_mfma_f32_16x16x32_bf16 v[40:43], v[104:107], v[136:139], v[40:43]\n"
    "ds_read_b128 v[116:119], v19 offset:1024\n"
    "s_add_u32 m0, s84, 79872\n"
    "s_nop 0\n"
    "buffer_load_dwordx4 v7, s[8:11], s75 offen lds\n"
    "v_mfma_f32_16x16x32_bf16 v[60:63], v[108:111], v[136:139], v[60:63]\n"
    "ds_read_b128 v[120:123], v19 offset:2048\n"
    "v_mfma_f32_16x16x32_bf16 v[44:47], v[104:107], v[140:143], v[44:47]\n"
    "ds_read_b128 v[124:127], v19 offset:3072\n"
    "s_add_u32 m0, s84, 80896\n"
    "s_nop 0\n"
    "buffer_load_dwordx4 v8, s[8:11], s75 offen lds\n"
    "v_mfma_f32_16x16x32_bf16 v[64:67], v[108:111], v[140:143], v[64:67]\n"
    "ds_read_b128 v[128:131], v19 offset:4096\n"
    "v_mfma_f32_16x16x32_bf16 v[48:51], v[104:107], v[144:147], v[48:51]\n"
    "s_add_u32 m0, s85, 94208\n"
    "s_nop 0\n"
    "buffer_load_dwordx4 v9, s[12:15], s76 offen lds\n"
    "v_mfma_f32_16x16x32_bf16 v[68:71], v[108:111], v[144:147], v[68:71]\n"
    "v_mfma_f32_16x16x32_bf16 v[52:55], v[104:107], v[148:151], v[52:55]\n"
    "v_mfma_f32_16x16x32_bf16 v[72:75], v[108:111], v[148:151], v[72:75]\n"
    "s_waitcnt lgkmcnt(0)\n"
    "v_mfma_f32_16x16x32_bf16 v[36:39], v[96:99], v[112:115], v[36:39]\n"
    "s_add_u32 m0, s85, 99328\n"
    "s_nop 0\n"
    "buffer_load_dwordx4 v10, s[16:19], s76 offen lds\n"
    "ds_read_b128 v[104:107], v16 offset:0\n"
    "ds_read_b128 v[132:135], v20 offset:0\n"
    "v_mfma_f32_16x16x32_bf16 v[56:59], v[100:103], v[112:115], v[56:59]\n"
    "s_cmp_lt_u32 s68, 2\n"
    "s_cbranch_scc0 Lk_skip_7\n"
    "s_add_u32 m0, s51, 98304\n"
    "s_nop 0\n"
    "buffer_load_dwordx4 v11, s[80:83], s78 offen lds\n"
    "Lk_skip_7:\n"
    "ds_read_b128 v[108:111], v16 offset:512\n"
    "v_mfma_f32_16x16x32_bf16 v[40:43], v[96:99], v[116:119], v[40:43]\n"
    "ds_read_b128 v[136:139], v20 offset:1024\n"
    "s_add_u32 m0, s50, 104448\n"
    "s_nop 0\n"
    "buffer_load_dwordx4 v12, s[20:23], s77 offen lds\n"
    "v_mfma_f32_16x16x32_bf16 v[60:63], v[100:103], v[116:119], v[60:63]\n"
    "ds_read_b128 v[140:143], v20 offset:2048\n"
    "v_mfma_f32_16x16x32_bf16 v[44:47], v[96:99], v[120:123], v[44:47]\n"
    "ds_read_b128 v[144:147], v20 offset:3072\n"
    "s_add_u32 m0, s50, 105472\n"
    "s_nop 0\n"
    "buffer_load_dwordx4 v13, s[20:23], s77 offen lds\n"
    "v_mfma_f32_16x16x32_bf16 v[64:67], v[100:103], v[120:123], v[64:67]\n"
    "ds_read_b128 v[148:151], v20 offset:4096\n"
    "v_mfma_f32_16x16x32_bf16 v[48:51], v[96:99], v[124:127], v[48:51]\n"
    "s_add_u32 m0, s50, 106496\n"
    "s_nop 0\n"
    "buffer_load_dwordx4 v14, s[20:23], s77 offen lds\n"
    "v_mfma_f32_16x16x32_bf16 v[68:71], v[100:103], v[124:127], v[68:71]\n"
    "v_mfma_f32_16x16x32_bf16 v[52:55], v[96:99], v[128:131], v[52:55]\n"
    "v_mfma_f32_16x16x32_bf16 v[72:75], v[100:103], v[128:131], v[72:75]\n"
    "s_waitcnt vmcnt(9) lgkmcnt(0)\n"
    "s_barrier\n"
    "v_mfma_f32_16x16x32_bf16 v[36:39], v[104:107], v[132:135], v[36:39]\n"
    "s_add_u32 s75, s4, s74\n"
    "s_add_u32 s76, s5, s74\n"
    "s_add_u32 s78, s3, s74\n"
    "s_mul_i32 s77, s74, 1536\n"
    "s_add_u32 s77, s77, s2\n"
    "s_add_u32 s74, s74, 128\n"
    "s_cmp_eq_u32 s74, 1280\n"
    "s_cselect_b32 s74, 0, s74\n"
    "s_add_u32 m0, s84, 0\n"
    "s_nop 0\n"
    "buffer_load_dwordx4 v5, s[8:11], s75 offen lds\n"
    "ds_read_b128 v[96:99], v15 offset:38912\n"
    "ds_read_b128 v[112:115], v19 offset:38912\n"
    "v_mfma_f32_16x16x32_bf16 v[56:59], v[108:111], v[132:135], v[56:59]\n"
    "s_add_u32 m0, s84, 1024\n"
    "s_nop 0\n"
    "buffer_load_dwordx4 v6, s[8:11], s75 offen lds\n"
    "ds_read_b128 v[100:103], v15 offset:39424\n"
    "v_mfma_f32_16x16x32_bf16 v[40:43], v[104:107], v[136:139], v[40:43]\n"
    "ds_read_b128 v[116:119], v19 offset:39936\n"
    "s_add_u32 m0, s84, 2048\n"
    "s_nop 0\n"
    "buffer_load_dwordx4 v7, s[8:11], s75 offen lds\n"
    "v_mfma_f32_16x16x32_bf16 v[60:63], v[108:111], v[136:139], v[60:63]\n"
    "ds_read_b128 v[120:123], v19 offset:40960\n"
    "v_mfma_f32_16x16x32_bf16 v[44:47], v[104:107], v[140:143], v[44:47]\n"
    "ds_read_b128 v[124:127], v19 offset:41984\n"
    "s_add_u32 m0, s84, 3072\n"
    "s_nop 0\n"
    "buffer_load_dwordx4 v8, s[8:11], s75 offen lds\n"
    "v_mfma_f32_16x16x32_bf16 v[64:67], v[108:111], v[140:143], v[64:67]\n"
    "ds_read_b128 v[128:131], v19 offset:43008\n"
    "v_mfma_f32_16x16x32_bf16 v[48:51], v[104:107], v[144:147], v[48:51]\n"
    "s_add_u32 m0, s85, 16384\n"
    "s_nop 0\n"
    "buffer_load_dwordx4 v9, s[12:15], s76 offen lds\n"
    "v_mfma_f32_16x16x32_bf16 v[68:71], v[108:111], v[144:147], v[68:71]\n"
    "v_mfma_f32_16x16x32_bf16 v[52:55], v[104:107], v[148:151], v[52:55]\n"
    "v_mfma_f32_16x16x32_bf16 v[72:75], v[108:111], v[148:151], v[72:75]\n"
    "s_waitcnt lgkmcnt(0)\n"
    "v_mfma_f32_16x16x32_bf16 v[36:39], v[96:99], v[112:115], v[36:39]\n"
    "s_add_u32 m0, s85, 21504\n"
    "s_nop 0\n"
    "buffer_load_dwordx4 v10, s[16:19], s76 offen lds\n"
    "ds_read_b128 v[104:107], v16 offset:38912\n"
    "ds_read_b128 v[132:135], v20 offset:38912\n"
    "v_mfma_f32_16x16x32_bf16 v[56:59], v[100:103], v[112:115], v[56:59]\n"
    "s_cmp_lt_u32 s68, 2\n"
    "s_cbranch_scc0 Lk_skip_8\n"
    "s_add_u32 m0, s51, 20480\n"
    "s_nop 0\n"
    "buffer_load_dwordx4 v11, s[80:83], s78 offen lds\n"
    "Lk_skip_8:\n"
    "ds_read_b128 v[108:111], v16 offset:39424\n"
    "v_mfma_f32_16x16x32_bf16 v[40:43], v[96:99], v[116:119], v[40:43]\n"
    "ds_read_b128 v[136:139], v20 offset:39936\n"
    "s_add_u32 m0, s50, 26624\n"
    "s_nop 0\n"
    "buffer_load_dwordx4 v12, s[20:23], s77 offen lds\n"
    "v_mfma_f32_16x16x32_bf16 v[60:63], v[100:103], v[116:119], v[60:63]\n"
    "ds_read_b128 v[140:143], v20 offset:40960\n"
    "v_mfma_f32_16x16x32_bf16 v[44:47], v[96:99], v[120:123], v[44:47]\n"
    "ds_read_b128 v[144:147], v20 offset:41984\n"
    "s_add_u32 m0, s50, 27648\n"
    "s_nop 0\n"
    "buffer_load_dwordx4 v13, s[20:23], s77 offen lds\n"
    "v_mfma_f32_16x16x32_bf16 v[64:67], v[100:103], v[120:123], v[64:67]\n"
    "ds_read_b128 v[148:151], v20 offset:43008\n"
    "v_mfma_f32_16x16x32_bf16 v[48:51], v[96:99], v[124:127], v[48:51]\n"
    "s_add_u32 m0, s50, 28672\n"
    "s_nop 0\n"
    "buffer_load_dwordx4 v14, s[20:23], s77 offen lds\n"
    "v_mfma_f32_16x16x32_bf16 v[68:71], v[100:103], v[124:127], v[68:71]\n"
    "v_mfma_f32_16x16x32_bf16 v[52:55], v[96:99], v[128:131], v[52:55]\n"
    "v_mfma_f32_16x16x32_bf16 v[72:75], v[100:103], v[128:131], v[72:75]\n"
    "s_waitcnt vmcnt(9) lgkmcnt(0)\n"
    "s_barrier\n"
    "v_mfma_f32_16x16x32_bf16 v[36:39], v[104:107], v[132:135], v[36:39]\n"
    "s_add_u32 s75, s4, s74\n"
    "s_add_u32 s76, s5, s74\n"
    "s_add_u32 s78, s3, s74\n"
    "s_mul_i32 s77, s74, 1536\n"
    "s_add_u32 s77, s77, s2\n"
    "s_add_u32 s74, s74, 128\n"
    "s_cmp_eq_u32 s74, 1280\n"
    "s_cselect_b32 s74, 0, s74\n"
    "s_add_u32 m0, s84, 38912\n"
    "s_nop 0\n"
    "buffer_load_dwordx4 v5, s[8:11], s75 offen lds\n"
    "ds_read_b128 v[96:99], v17 offset:0\n"
    "ds_read_b128 v[112:115], v21 offset:0\n"
    "v_mfma_f32_16x16x32_bf16 v[56:59], v[108:111], v[132:135], v[56:59]\n"
    "s_add_u32 m0, s84, 39936\n"
    "s_nop 0\n"
    "buffer_load_dwordx4 v6, s[8:11], s75 offen lds\n"
    "ds_read_b128 v[100:103], v17 offset:512\n"
    "v_mfma_f32_16x16x32_bf16 v[40:43], v[104:107], v[136:139], v[40:43]\n"
    "ds_read_b128 v[116:119], v21 offset:1024\n"
    "s_add_u32 m0, s84, 40960\n"
    "s_nop 0\n"
    "buffer_load_dwordx4 v7, s[8:11], s75 offen lds\n"
    "v_mfma_f32_16x16x32_bf16 v[60:63], v[108:111], v[136:139], v[60:63]\n"
    "ds_read_b128 v[120:123], v21 offset:2048\n"
    "v_mfma_f32_16x16x32_bf16 v[44:47], v[104:107], v[140:143], v[44:47]\n"
    "ds_read_b128 v[124:127], v21 offset:3072\n"
    "s_add_u32 m0, s84, 41984\n"
    "s_nop 0\n"
    "buffer_load_dwordx4 v8, s[8:11], s75 offen lds\n"
    "v_mfma_f32_16x16x32_bf16 v[64:67], v[108:111], v[140:143], v[64:67]\n"
    "ds_read_b128 v[128:131], v21 offset:4096\n"
    "v_mfma_f32_16x16x32_bf16 v[48:51], v[104:107], v[144:147], v[48:51]\n"
    "s_add_u32 m0, s85, 55296\n"
    "s_nop 0\n"
    "buffer_load_dwordx4 v9, s[12:15], s76 offen lds\n"
    "v_mfma_f32_16x16x32_bf16 v[68:71], v[108:111], v[144:147], v[68:71]\n"
    "v_mfma_f32_16x16x32_bf16 v[52:55], v[104:107], v[148:151], v[52:55]\n"
    "v_mfma_f32_16x16x32_bf16 v[72:75], v[108:111], v[148:151], v[72:75]\n"
    "s_waitcnt lgkmcnt(0)\n"
    "v_mfma_f32_16x16x32_bf16 v[36:39], v[96:99], v[112:115], v[36:39]\n"
    "s_add_u32 m0, s85, 60416\n"
    "s_nop 0\n"
    "buffer_load_dwordx4 v10, s[16:19], s76 offen lds\n"
    "ds_read_b128 v[104:107], v18 offset:0\n"
    "ds_read_b128 v[132:135], v22 offset:0\n"
    "v_mfma_f32_16x16x32_bf16 v[56:59], v[100:103], v[112:115], v[56:59]\n"
    "s_cmp_lt_u32 s68, 2\n"
    "s_cbranch_scc0 Lk_skip_9\n"
    "s_add_u32 m0, s51, 59392\n"
    "s_nop 0\n"
    "buffer_load_dwordx4 v11, s[80:83], s78 offen lds\n"
    "Lk_skip_9:\n"
    "ds_read_b128 v[108:111], v18 offset:512\n"
    "v_mfma_f32_16x16x32_bf16 v[40:43], v[96:99], v[116:119], v[40:43]\n"
    "ds_read_b128 v[136:139], v22 offset:1024\n"
    "s_add_u32 m0, s50, 65536\n"
    "s_nop 0\n"
    "buffer_load_dwordx4 v12, s[20:23], s77 offen lds\n"
    "v_mfma_f32_16x16x32_bf16 v[60:63], v[100:103], v[116:119], v[60:63]\n"
    "ds_read_b128 v[140:143], v22 offset:2048\n"
    "v_mfma_f32_16x16x32_bf16 v[44:47], v[96:99], v[120:123], v[44:47]\n"
    "ds_read_b128 v[144:147], v22 offset:3072\n"
    "s_add_u32 m0, s50, 66560\n"
    "s_nop 0\n"
    "buffer_load_dwordx4 v13, s[20:23], s77 offen lds\n"
    "v_mfma_f32_16x16x32_bf16 v[64:67], v[100:103], v[120:123], v[64:67]\n"
    "ds_read_b128 v[148:151], v22 offset:4096\n"
    "v_mfma_f32_16x16x32_bf16 v[48:51], v[96:99], v[124:127], v[48:51]\n"
    "s_add_u32 m0, s50, 67584\n"
    "s_nop 0\n"
    "buffer_load_dwordx4 v14, s[20:23], s77 offen lds\n"
    "v_mfma_f32_16x16x32_bf16 v[68:71], v[100:103], v[124:127], v[68:71]\n"
    "v_mfma_f32_16x16x32_bf16 v[52:55], v[96:99], v[128:131], v[52:55]\n"
    "v_mfma_f32_16x16x32_bf16 v[72:75], v[100:103], v[128:131], v[72:75]\n"
    "s_waitcnt vmcnt(9) lgkmcnt(0)\n"
    "s_barrier\n"
    "v_mfma_f32_16x16x32_bf16 v[36:39], v[104:107], v[132:135], v[36:39]\n"
    "buffer_load_dwordx4 v[152:155], v168, s[24:27], s63 offen sc1\n"
    "ds_read_b128 v[96:99], v15 offset:0\n"
    "buffer_load_dwordx4 v[156:159], v169, s[24:27], s60 offen sc1\n"
    "ds_read_b128 v[112:115], v19 offset:0\n"
    "v_mfma_f32_16x16x32_bf16 v[56:59], v[108:111], v[132:135], v[56:59]\n"
    "buffer_load_dwordx4 v[160:163], v169, s[24:27], s61 offen sc1\n"
    "ds_read_b128 v[100:103], v15 offset:512\n"
    "v_mfma_f32_16x16x32_bf16 v[40:43], v[104:107], v[136:139], v[40:43]\n"
    "buffer_load_dwordx4 v[164:167], v170, s[24:27], s62 offen sc1\n"
    "ds_read_b128 v[116:119], v19 offset:1024\n"
    "v_mfma_f32_16x16x32_bf16 v[60:63], v[108:111], v[136:139], v[60:63]\n"
    "s_add_u32 s75, s4, s74\n"
    "s_add_u32 s76, s5, s74\n"
    "s_add_u32 s78, s3, s74\n"
    "s_mul_i32 s77, s74, 1536\n"
    "s_add_u32 s77, s77, s2\n"
    "s_add_u32 s74, s74, 128\n"
    "s_cmp_eq_u32 s74, 1280\n"
    "s_cselect_b32 s74, 0, s74\n"
    "s_add_u32 m0, s84, 77824\n"
    "s_nop 0\n"
    "buffer_load_dwordx4 v5, s[8:11], s75 offen lds\n"
    "ds_read_b128 v[120:123], v19 offset:2048\n"
    "v_mfma_f32_16x16x32_bf16 v[44:47], v[104:107], v[140:143], v[44:47]\n"
    "s_add_u32 m0, s84, 78848\n"
    "s_nop 0\n"
    "buffer_load_dwordx4 v6, s[8:11], s75 offen lds\n"
    "ds_read_b128 v[124:127], v19 offset:3072\n"
    "v_mfma_f32_16x16x32_bf16 v[64:67], v[108:111], v[140:143], v[64:67]\n"
    "s_add_u32 m0, s84, 79872\n"
    "s_nop 0\n"
    "buffer_load_dwordx4 v7, s[8:11], s75 offen lds\n"
    "ds_read_b128 v[128:131], v19 offset:4096\n"
    "v_mfma_f32_16x16x32_bf16 v[48:51], v[104:107], v[144:147], v[48:51]\n"
    "s_add_u32 m0, s84, 80896\n"
    "s_nop 0\n"
    "buffer_load_dwordx4 v8, s[8:11], s75 offen lds\n"
    "v_mfma_f32_16x16x32_bf16 v[68:71], v[108:111], v[144:147], v[68:71]\n"
    "v_mfma_f32_16x16x32_bf16 v[52:55], v[104:107], v[148:151], v[52:55]\n"
    "s_add_u32 m0, s85, 94208\n"
    "s_nop 0\n"
    "buffer_load_dwordx4 v9, s[12:15], s76 offen lds\n"
    "v_mfma_f32_16x16x32_bf16 v[72:75], v[108:111], v[148:151], v[72:75]\n"
    "s_waitcnt lgkmcnt(0)\n"
    "v_mfma_f32_16x16x32_bf16 v[36:39], v[96:99], v[112:115], v[36:39]\n"
    "s_add_u32 m0, s85, 99328\n"
    "s_nop 0\n"
    "buffer_load_dwordx4 v10, s[16:19], s76 offen lds\n"
    "ds_read_b128 v[104:107], v16 offset:0\n"
    "ds_read_b128 v[132:135], v20 offset:0\n"
    "v_mfma_f32_16x16x32_bf16 v[56:59], v[100:103], v[112:115], v[56:59]\n"
    "s_cmp_lt_u32 s68, 2\n"
    "s_cbranch_scc0 Lk_skip_10\n"
    "s_add_u32 m0, s51, 98304\n"
    "s_nop 0\n"
    "buffer_load_dwordx4 v11, s[80:83], s78 offen lds\n"
    "Lk_skip_10:\n"
    "ds_read_b128 v[108:111], v16 offset:512\n"
    "v_mfma_f32_16x16x32_bf16 v[40:43], v[96:99], v[116:119], v[40:43]\n"
    "ds_read_b128 v[136:139], v20 offset:1024\n"
    "s_add_u32 m0, s50, 104448\n"
    "s_nop 0\n"
    "buffer_load_dwordx4 v12, s[20:23], s77 offen lds\n"
    "v_mfma_f32_16x16x32_bf16 v[60:63], v[100:103], v[116:119], v[60:63]\n"
    "ds_read_b128 v[140:143], v20 offset:2048\n"
    "v_mfma_f32_16x16x32_bf16 v[44:47], v[96:99], v[120:123], v[44:47]\n"
    "ds_read_b128 v[144:147], v20 offset:3072\n"
    "s_add_u32 m0, s50, 105472\n"
    "s_nop 0\n"
    "buffer_load_dwordx4 v13, s[20:23], s77 offen lds\n"
    "v_mfma_f32_16x16x32_bf16 v[64:67], v[100:103], v[120:123], v[64:67]\n"
    "ds_read_b128 v[148:151], v20 offset:4096\n"
    "v_mfma_f32_16x16x32_bf16 v[48:51], v[96:99], v[124:127], v[48:51]\n"
    "s_add_u32 m0, s50, 106496\n"
    "s_nop 0\n"
    "buffer_load_dwordx4 v14, s[20:23], s77 offen lds\n"
    "v_mfma_f32_16x16x32_bf16 v[68:71], v[100:103], v[124:127], v[68:71]\n"
    "v_mfma_f32_16x16x32_bf16 v[52:55], v[96:99], v[128:131], v[52:55]\n"
    "v_mfma_f32_16x16x32_bf16 v[72:75], v[100:103], v[128:131], v[72:75]\n"
    "s_waitcnt vmcnt(13) lgkmcnt(0)\n"
    "s_barrier\n"
    "v_mfma_f32_16x16x32_bf16 v[36:39], v[104:107], v[132:135], v[36:39]\n"
    "s_add_u32 s75, s4, s74\n"
    "s_add_u32 s76, s5, s74\n"
    "s_add_u32 s78, s3, s74\n"
    "s_mul_i32 s77, s74, 1536\n"
    "s_add_u32 s77, s77, s2\n"
    "s_add_u32 s74, s74, 128\n"
    "s_cmp_eq_u32 s74, 1280\n"
    "s_cselect_b32 s74, 0, s74\n"
    "s_add_u32 m0, s84, 0\n"
    "s_nop 0\n"
    "buffer_load_dwordx4 v5, s[8:11], s75 offen lds\n"
    "ds_read_b128 v[96:99], v15 offset:38912\n"
    "ds_read_b128 v[112:115], v19 offset:38912\n"
    "v_mfma_f32_16x16x32_bf16 v[56:59], v[108:111], v[132:135], v[56:59]\n"
    "s_add_u32 m0, s84, 1024\n"
    "s_nop 0\n"
    "buffer_load_dwordx4 v6, s[8:11], s75 offen lds\n"
    "ds_read_b128 v[100:103], v15 offset:39424\n"
    "v_mfma_f32_16x16x32_bf16 v[40:43], v[104:107], v[136:139], v[40:43]\n"
    "ds_read_b128 v[116:119], v19 offset:39936\n"
    "s_add_u32 m0, s84, 2048\n"
    "s_nop 0\n"
    "buffer_load_dwordx4 v7, s[8:11], s75 offen lds\n"
    "v_mfma_f32_16x16x32_bf16 v[60:63], v[108:111], v[136:139], v[60:63]\n"
    "ds_read_b128 v[120:123], v19 offset:40960\n"
    "v_mfma_f32_16x16x32_bf16 v[44:47], v[104:107], v[140:143], v[44:47]\n"
    "ds_read_b128 v[124:127], v19 offset:41984\n"
    "s_add_u32 m0, s84, 3072\n"
    "s_nop 0\n"
    "buffer_load_dwordx4 v8, s[8:11], s75 offen lds\n"
    "v_mfma_f32_16x16x32_bf16 v[64:67], v[108:111], v[140:143], v[64:67]\n"
    "ds_read_b128 v[128:131], v19 offset:43008\n"
    "v_mfma_f32_16x16x32_bf16 v[48:51], v[104:107], v[144:147], v[48:51]\n"
    "s_add_u32 m0, s85, 16384\n"
    "s_nop 0\n"
    "buffer_load_dwordx4 v9, s[12:15], s76 offen lds\n"
    "v_mfma_f32_16x16x32_bf16 v[68:71], v[108:111], v[144:147], v[68:71]\n"
    "v_mfma_f32_16x16x32_bf16 v[52:55], v[104:107], v[148:151], v[52:55]\n"
    "v_mfma_f32_16x16x32_bf16 v[72:75], v[108:111], v[148:151], v[72:75]\n"
    "s_waitcnt lgkmcnt(0)\n"
    "v_mfma_f32_16x16x32_bf16 v[36:39], v[96:99], v[112:115], v[36:39]\n"
    "s_waitcnt vmcnt(14)\n"
    "v_xor_b32 v29, s73, v165\n"
    "s_add_u32 m0, s85, 21504\n"
    "s_nop 0\n"
    "buffer_load_dwordx4 v10, s[16:19], s76 offen lds\n"
    "ds_read_b128 v[104:107], v16 offset:38912\n"
    "v_xor_b32 v28, s73, v167\n"
    "ds_read_b128 v[132:135], v20 offset:38912\n"
    "v_or_b32 v28, v28, v29\n"
    "v_mfma_f32_16x16x32_bf16 v[56:59], v[100:103], v[112:115], v[56:59]\n"
    "v_cndmask_b32 v28, 0, v28, s[86:87]\n"
    "s_cmp_lt_u32 s68, 2\n"
    "s_cbranch_scc0 Lk_skip_11\n"
    "s_add_u32 m0, s51, 20480\n"
    "s_nop 0\n"
    "buffer_load_dwordx4 v11, s[80:83], s78 offen lds\n"
    "Lk_skip_11:\n"
    "ds_read_b128 v[108:111], v16 offset:39424\n"
    "v_xor_b32 v29, s73, v157\n"
    "v_mfma_f32_16x16x32_bf16 v[40:43], v[96:99], v[116:119], v[40:43]\n"
    "ds_read_b128 v[136:139], v20 offset:39936\n"
    "v_xor_b32 v30, s73, v159\n"
    "s_add_u32 m0, s50, 26624\n"
    "s_nop 0\n"
    "buffer_load_dwordx4 v12, s[20:23], s77 offen lds\n"
    "v_mfma_f32_16x16x32_bf16 v[60:63], v[100:103], v[116:119], v[60:63]\n"
    "v_or3_b32 v28, v28, v29, v30\n"
    "ds_read_b128 v[140:143], v20 offset:40960\n"
    "v_xor_b32 v29, s73, v161\n"
    "v_mfma_f32_16x16x32_bf16 v[44:47], v[96:99], v[120:123], v[44:47]\n"
    "v_xor_b32 v30, s73, v163\n"
    "ds_read_b128 v[144:147], v20 offset:41984\n"
    "v_or3_b32 v28, v28, v29, v30\n"
    "s_add_u32 m0, s50, 27648\n"
    "s_nop 0\n"
    "buffer_load_dwordx4 v13, s[20:23], s77 offen lds\n"
    "v_mfma_f32_16x16x32_bf16 v[64:67], v[100:103], v[120:123], v[64:67]\n"
    "ds_read_b128 v[148:151], v20 offset:43008\n"
    "v_xor_b32 v29, s73, v153\n"
    "v_mfma_f32_16x16x32_bf16 v[48:51], v[96:99], v[124:127], v[48:51]\n"
    "v_xor_b32 v30, s73, v155\n"
    "s_add_u32 m0, s50, 28672\n"
    "s_nop 0\n"
    "buffer_load_dwordx4 v14, s[20:23], s77 offen lds\n"
    "v_mfma_f32_16x16x32_bf16 v[68:71], v[100:103], v[124:127], v[68:71]\n"
    "v_or3_b32 v28, v28, v29, v30\n"
    "v_mfma_f32_16x16x32_bf16 v[52:55], v[96:99], v[128:131], v[52:55]\n"
    "v_cmp_ne_u32 vcc, 0, v28\n"
    "v_mfma_f32_16x16x32_bf16 v[72:75], v[100:103], v[128:131], v[72:75]\n"
    "s_nop 4\n"
    "s_cbranch_vccz Lk_skip_12\n"
    "s_mov_b32 s0, 131072\n"
    "s_nop 4\n"
    "Lk_poll_13:\n"
    "buffer_load_dwordx4 v[152:155], v168, s[24:27], s63 offen sc1\n"
    "buffer_load_dwordx4 v[156:159], v169, s[24:27], s60 offen sc1\n"
    "buffer_load_dwordx4 v[160:163], v169, s[24:27], s61 offen sc1\n"
    "buffer_load_dwordx4 v[164:167], v170, s[24:27], s62 offen sc1\n"
    "Lk_chk_15:\n"
    "s_waitcnt vmcnt(0)\n"
    "v_xor_b32 v29, s73, v165\n"
    "v_xor_b32 v28, s73, v167\n"
    "v_or_b32 v28, v28, v29\n"
    "v_cndmask_b32 v28, 0, v28, s[86:87]\n"
    "v_xor_b32 v29, s73, v157\n"
    "v_xor_b32 v30, s73, v159\n"
    "v_or3_b32 v28, v28, v29, v30\n"
    "v_xor_b32 v29, s73, v161\n"
    "v_xor_b32 v30, s73, v163\n"
    "v_or3_b32 v28, v28, v29, v30\n"
    "v_xor_b32 v29, s73, v153\n"
    "v_xor_b32 v30, s73, v155\n"
    "v_or3_b32 v28, v28, v29, v30\n"
    "v_cmp_ne_u32 vcc, 0, v28\n"
    "s_nop 4\n"
    "s_cbranch_vccz Lk_done_14\n"
    "s_sleep 4\n"
    "s_sub_u32 s0, s0, 1\n"
    "s_cbranch_scc0 Lk_poll_13\n"
    "Lk_done_14:\n"
    "Lk_skip_12:\n"
    "s_waitcnt vmcnt(9) lgkmcnt(0)\n"
    "s_barrier\n"
    "v_mfma_f32_16x16x32_bf16 v[36:39], v[104:107], v[132:135], v[36:39]\n"
    "v_add_f32 v156, v156, v160\n"
    "v_add_f32 v153, 0x3727c5ac, v154\n"
    "ds_read_b128 v[96:99], v17 offset:0\n"
    "v_add_f32 v158, v158, v162\n"
    "v_rsq_f32 v153, v153\n"
    "ds_read_b128 v[112:115], v21 offset:0\n"
    "v_add_f32 v156, v156, v164\n"
    "v_add_f32 v157, v158, v166\n"
    "v_mfma_f32_16x16x32_bf16 v[56:59], v[108:111], v[132:135], v[56:59]\n"
    "v_mov_b32 v155, 0x3727c5ac\n"
    "ds_read_b128 v[100:103], v17 offset:512\n"
    "v_mov_b32 v158, v156\n"
    "v_mov_b32 v159, v157\n"
    "v_mfma_f32_16x16x32_bf16 v[40:43], v[104:107], v[136:139], v[40:43]\n"
    "v_mul_f32 v154, v154, v153\n"
    "v_permlane16_swap_b32 v156, v158\n"
    "ds_read_b128 v[116:119], v21 offset:1024\n"
    "v_fmac_f32 v155, v154, v153\n"
    "v_mfma_f32_16x16x32_bf16 v[60:63], v[108:111], v[136:139], v[60:63]\n"
    "v_permlane16_swap_b32 v157, v159\n"
    "v_add_f32 v156, v156, v158\n"
    "ds_read_b128 v[120:123], v21 offset:2048\n"
    "v_rsq_f32 v155, v155\n"
    "v_add_f32 v157, v157, v159\n"
    "v_mfma_f32_16x16x32_bf16 v[44:47], v[104:107], v[140:143], v[44:47]\n"
    "v_mov_b32 v158, v156\n"
    "v_mul_f32 v154, v155, v153\n"
    "ds_read_b128 v[124:127], v21 offset:3072\n"
    "v_mov_b32 v159, v157\n"
    "v_mfma_f32_16x16x32_bf16 v[64:67], v[108:111], v[140:143], v[64:67]\n"
    "v_rcp_f32 v155, v154\n"
    "v_permlane32_swap_b32 v156, v158\n"
    "ds_read_b128 v[128:131], v21 offset:4096\n"
    "v_permlane32_swap_b32 v157, v159\n"
    "v_xor_b32 v174, 0x80000000, v152\n"
    "v_mfma_f32_16x16x32_bf16 v[48:51], v[104:107], v[144:147], v[48:51]\n"
    "v_add_f32 v156, v156, v158\n"
    "v_mfma_f32_16x16x32_bf16 v[68:71], v[108:111], v[144:147], v[68:71]\n"
    "v_add_f32 v157, v157, v159\n"
    "v_and_b32 v177, s1, v155\n"
    "v_mfma_f32_16x16x32_bf16 v[52:55], v[104:107], v[148:151], v[52:55]\n"
    "v_and_b32 v161, s1, v156\n"
    "v_and_b32 v165, s1, v157\n"
    "v_mfma_f32_16x16x32_bf16 v[72:75], v[108:111], v[148:151], v[72:75]\n"
    "v_and_b32 v176, s1, v174\n"
    "s_waitcnt lgkmcnt(0)\n"
    "v_mfma_f32_16x16x32_bf16 v[36:39], v[96:99], v[112:115], v[36:39]\n"
    "v_lshlrev_b32 v5, 3, v1\n"
    "v_sub_f32 v162, v156, v161\n"
    "v_sub_f32 v178, v155, v177\n"
    "ds_read_b128 v[104:107], v18 offset:0\n"
    "v_sub_f32 v166, v157, v165\n"
    "v_and_b32 v29, s1, v162\n"
    "ds_read_b128 v[132:135], v22 offset:0\n"
    "v_sub_f32 v174, v174, v176\n"
    "v_and_b32 v30, s1, v166\n"
    "v_mfma_f32_16x16x32_bf16 v[56:59], v[100:103], v[112:115], v[56:59]\n"
    "v_cvt_pk_bf16_f32 v160, v161, v29\n"
    "ds_read_b128 v[108:111], v18 offset:512\n"
    "v_cvt_pk_bf16_f32 v178, v178, v176\n"
    "v_cvt_pk_bf16_f32 v164, v165, v30\n"
    "v_mfma_f32_16x16x32_bf16 v[40:43], v[96:99], v[116:119], v[40:43]\n"
    "v_and_b32 v175, s1, v174\n"
    "v_cvt_pk_bf16_f32 v163, v29, v161\n"
    "ds_read_b128 v[136:139], v22 offset:1024\n"
    "v_cvt_pk_bf16_f32 v167, v30, v165\n"
    "v_mfma_f32_16x16x32_bf16 v[60:63], v[100:103], v[116:119], v[60:63]\n"
    "v_cvt_pk_bf16_f32 v176, v176, v176\n"
    "v_sub_f32 v162, v162, v29\n"
    "ds_read_b128 v[140:143], v22 offset:2048\n"
    "v_add_u32 v5, 139008, v5\n"
    "v_sub_f32 v166, v166, v30\n"
    "v_sub_f32 v174, v174, v175\n"
    "v_mfma_f32_16x16x32_bf16 v[44:47], v[96:99], v[120:123], v[44:47]\n"
    "v_lshrrev_b32 v161, 16, v156\n"
    "v_cvt_pk_bf16_f32 v177, v175, v177\n"
    "ds_read_b128 v[144:147], v22 offset:3072\n"
    "v_lshrrev_b32 v165, 16, v157\n"
    "v_mfma_f32_16x16x32_bf16 v[64:67], v[100:103], v[120:123], v[64:67]\n"
    "v_and_b32 v162, s1, v162\n"
    "v_cvt_pk_bf16_f32 v179, v175, v174\n"
    "ds_read_b128 v[148:151], v22 offset:4096\n"
    "v_and_b32 v166, s1, v166\n"
    "ds_write_b128 v171, v[160:163] offset:0\n"
    "v_mfma_f32_16x16x32_bf16 v[48:51], v[96:99], v[124:127], v[48:51]\n"
    "ds_write_b128 v173, v[176:179]\n"
    "v_mfma_f32_16x16x32_bf16 v[68:71], v[100:103], v[124:127], v[68:71]\n"
    "ds_write_b128 v171, v[164:167] offset:16\n"
    "ds_write_b32 v172, v152 offset:0\n"
    "v_mfma_f32_16x16x32_bf16 v[52:55], v[96:99], v[128:131], v[52:55]\n"
    "ds_write_b32 v172, v153 offset:512\n"
    "ds_write_b32 v172, v154 offset:1024\n"
    "v_mfma_f32_16x16x32_bf16 v[72:75], v[100:103], v[128:131], v[72:75]\n"
    "ds_write_b32 v172, v155 offset:1536\n"
    "s_waitcnt vmcnt(0) lgkmcnt(0)\n"
    "s_barrier\n"
    "ds_read_b128 v[24:27], v34 offset:0\n"
    "ds_read_b128 v[28:31], v34 offset:256\n"
    "ds_read_b128 v[76:79], v23 offset:0\n"
    "ds_read_b128 v[80:83], v23 offset:256\n"
    "ds_read_b128 v[84:87], v23 offset:512\n"
    "ds_read_b128 v[88:91], v23 offset:768\n"
    "ds_read_b128 v[92:95], v23 offset:1024\n"
    "ds_read_b128 v[6:9], v35 offset:512\n"
    "ds_read_b128 v[10:13], v35 offset:576\n"
    "v_mfma_f32_16x16x32_bf16 v[36:39], v[104:107], v[132:135], v[36:39]\n"
    "buffer_load_dwordx4 v[152:155], v3, s[24:27], s52 offen offset:0 sc1\n"
    "ds_read_b128 v[96:99], v15 offset:0\n"
    "ds_read_b128 v[112:115], v19 offset:0\n"
    "v_mfma_f32_16x16x32_bf16 v[56:59], v[108:111], v[132:135], v[56:59]\n"
    "buffer_load_dwordx4 v[156:159], v3, s[24:27], s52 offen offset:1024 sc1\n"
    "ds_read_b128 v[100:103], v15 offset:512\n"
    "v_mfma_f32_16x16x32_bf16 v[40:43], v[104:107], v[136:139], v[40:43]\n"
    "ds_read_b128 v[116:119], v19 offset:1024\n"
    "buffer_load_dwordx4 v[160:163], v3, s[24:27], s52 offen offset:2048 sc1\n"
    "v_mfma_f32_16x16x32_bf16 v[60:63], v[108:111], v[136:139], v[60:63]\n"
    "ds_read_b128 v[120:123], v19 offset:2048\n"
    "v_mfma_f32_16x16x32_bf16 v[44:47], v[104:107], v[140:143], v[44:47]\n"
    "ds_read_b128 v[124:127], v19 offset:3072\n"
    "buffer_load_dwordx4 v[164:167], v3, s[24:27], s52 offen offset:3072 sc1\n"
    "v_mfma_f32_16x16x32_bf16 v[64:67], v[108:111], v[140:143], v[64:67]\n"
    "ds_read_b128 v[128:131], v19 offset:4096\n"
    "v_mfma_f32_16x16x32_bf16 v[48:51], v[104:107], v[144:147], v[48:51]\n"
    "s_add_u32 s95, s52, 4096\n"
    "buffer_load_dwordx4 v[168:171], v3, s[24:27], s95 offen sc1\n"
    "v_mfma_f32_16x16x32_bf16 v[68:71], v[108:111], v[144:147], v[68:71]\n"
    "v_mfma_f32_16x16x32_bf16 v[52:55], v[104:107], v[148:151], v[52:55]\n"
    "v_mfma_f32_16x16x32_bf16 v[72:75], v[108:111], v[148:151], v[72:75]\n"
    "s_waitcnt lgkmcnt(0)\n"
    "v_mfma_f32_16x16x32_bf16 v[36:39], v[96:99], v[112:115], v[36:39]\n"
    "buffer_load_dwordx4 v[172:175], v3, s[24:27], s53 offen offset:0 sc1\n"
    "ds_read_b128 v[104:107], v16 offset:0\n"
    "ds_read_b128 v[132:135], v20 offset:0\n"
    "v_mfma_f32_16x16x32_bf16 v[56:59], v[100:103], v[112:115], v[56:59]\n"
    "buffer_load_dwordx4 v[176:179], v3, s[24:27], s53 offen offset:1024 sc1\n"
    "ds_read_b128 v[108:111], v16 offset:512\n"
    "v_mfma_f32_16x16x32_bf16 v[40:43], v[96:99], v[116:119], v[40:43]\n"
    "ds_read_b128 v[136:139], v20 offset:1024\n"
    "buffer_load_dwordx4 v[180:183], v3, s[24:27], s53 offen offset:2048 sc1\n"
    "v_mfma_f32_16x16x32_bf16 v[60:63], v[100:103], v[116:119], v[60:63]\n"
    "ds_read_b128 v[140:143], v20 offset:2048\n"
    "v_mfma_f32_16x16x32_bf16 v[44:47], v[96:99], v[120:123], v[44:47]\n"
    "ds_read_b128 v[144:147], v20 offset:3072\n"
    "buffer_load_dwordx4 v[184:187], v3, s[24:27], s53 offen offset:3072 sc1\n"
    "v_mfma_f32_16x16x32_bf16 v[64:67], v[100:103], v[120:123], v[64:67]\n"
    "ds_read_b128 v[148:151], v20 offset:4096\n"
    "v_mfma_f32_16x16x32_bf16 v[48:51], v[96:99], v[124:127], v[48:51]\n"
    "s_add_u32 s95, s53, 4096\n"
    "buffer_load_dwordx4 v[188:191], v3, s[24:27], s95 offen sc1\n"
    "v_mfma_f32_16x16x32_bf16 v[68:71], v[100:103], v[124:127], v[68:71]\n"
    "v_mfma_f32_16x16x32_bf16 v[52:55], v[96:99], v[128:131], v[52:55]\n"
    "v_mfma_f32_16x16x32_bf16 v[72:75], v[100:103], v[128:131], v[72:75]\n"
    "s_waitcnt lgkmcnt(0)\n"
    "s_barrier\n"
    "v_mfma_f32_16x16x32_bf16 v[36:39], v[104:107], v[132:135], v[36:39]\n"
    "buffer_load_dwordx4 v[192:195], v3, s[24:27], s54 offen offset:0 sc1\n"
    "ds_read_b64 v[14:15], v5 offset:15360\n"
    "ds_read_b64 v[16:17], v5 offset:15488\n"
    "ds_read_b64 v[18:19], v5 offset:15616\n"
    "v_mfma_f32_16x16x32_bf16 v[56:59], v[108:111], v[132:135], v[56:59]\n"
    "ds_read_b64 v[20:21], v5 offset:15744\n"
    "buffer_load_dwordx4 v[196:199], v3, s[24:27], s54 offen offset:1024 sc1\n"
    "ds_read_b64 v[22:23], v5 offset:15872\n"
    "v_mfma_f32_16x16x32_bf16 v[40:43], v[104:107], v[136:139], v[40:43]\n"
    "ds_read_b64 v[122:123], v5 offset:16000\n"
    "ds_read_b64 v[124:125], v5 offset:16128\n"
    "ds_read_b64 v[126:127], v5 offset:16256\n"
    "buffer_load_dwordx4 v[200:203], v3, s[24:27], s54 offen offset:2048 sc1\n"
    "v_mfma_f32_16x16x32_bf16 v[60:63], v[108:111], v[136:139], v[60:63]\n"
    "ds_read_b64 v[128:129], v5 offset:16384\n"
    "ds_read_b64 v[130:131], v5 offset:16512\n"
    "v_mfma_f32_16x16x32_bf16 v[44:47], v[104:107], v[140:143], v[44:47]\n"
    "ds_read_b64 v[112:113], v5 offset:640\n"
    "buffer_load_dwordx4 v[204:207], v3, s[24:27], s54 offen offset:3072 sc1\n"
    "ds_read_b64 v[114:115], v5 offset:768\n"
    "ds_read_b64 v[116:117], v5 offset:896\n"
    "v_mfma_f32_16x16x32_bf16 v[64:67], v[108:111], v[140:143], v[64:67]\n"
    "ds_read_b64 v[118:119], v5 offset:1024\n"
    "ds_read_b64 v[120:121], v5 offset:1152\n"
    "s_add_u32 s95, s54, 4096\n"
    "buffer_load_dwordx4 v[208:211], v3, s[24:27], s95 offen sc1\n"
    "v_mfma_f32_16x16x32_bf16 v[48:51], v[104:107], v[144:147], v[48:51]\n"
    "v_mfma_f32_16x16x32_bf16 v[68:71], v[108:111], v[144:147], v[68:71]\n"
    "v_mfma_f32_16x16x32_bf16 v[52:55], v[104:107], v[148:151], v[52:55]\n"
    "v_mfma_f32_16x16x32_bf16 v[72:75], v[108:111], v[148:151], v[72:75]\n"
    "v_mfma_f32_16x16x32_bf16 v[36:39], v[24:27], v[76:79], v[36:39]\n"
    "v_mfma_f32_16x16x32_bf16 v[56:59], v[28:31], v[76:79], v[56:59]\n"
    "v_mfma_f32_16x16x32_bf16 v[40:43], v[24:27], v[80:83], v[40:43]\n"
    "v_mfma_f32_16x16x32_bf16 v[60:63], v[28:31], v[80:83], v[60:63]\n"
    "v_mfma_f32_16x16x32_bf16 v[44:47], v[24:27], v[84:87], v[44:47]\n"
    "v_mfma_f32_16x16x32_bf16 v[64:67], v[28:31], v[84:87], v[64:67]\n"
    "v_mfma_f32_16x16x32_bf16 v[48:51], v[24:27], v[88:91], v[48:51]\n"
    "v_mfma_f32_16x16x32_bf16 v[68:71], v[28:31], v[88:91], v[68:71]\n"
    "v_mfma_f32_16x16x32_bf16 v[52:55], v[24:27], v[92:95], v[52:55]\n"
    "v_mfma_f32_16x16x32_bf16 v[72:75], v[28:31], v[92:95], v[72:75]\n"
    "ds_read_b64 v[76:77], v5 offset:1280\n"
    "ds_read_b64 v[78:79], v5 offset:1408\n"
    "ds_read_b64 v[80:81], v5 offset:1536\n"
    "ds_read_b64 v[82:83], v5 offset:1664\n"
    "ds_read_b64 v[84:85], v5 offset:1792\n"
    "ds_read_b64 v[86:87], v5 offset:3200\n"
    "ds_read_b64 v[88:89], v5 offset:3328\n"
    "ds_read_b64 v[90:91], v5 offset:3456\n"
    "ds_read_b64 v[92:93], v5 offset:3584\n"
    "ds_read_b64 v[94:95], v5 offset:3712\n"
    "v_pk_mul_f32 v[36:37], v[36:37], v[6:7]\n"
    "v_pk_mul_f32 v[38:39], v[38:39], v[8:9]\n"
    "v_pk_mul_f32 v[56:57], v[56:57], v[10:11]\n"
    "v_pk_mul_f32 v[58:59], v[58:59], v[12:13]\n"
    "v_pk_mul_f32 v[40:41], v[40:41], v[6:7]\n"
    "v_pk_mul_f32 v[42:43], v[42:43], v[8:9]\n"
    "v_pk_mul_f32 v[60:61], v[60:61], v[10:11]\n"
    "v_pk_mul_f32 v[62:63], v[62:63], v[12:13]\n"
    "v_pk_mul_f32 v[44:45], v[44:45], v[6:7]\n"
    "v_pk_mul_f32 v[46:47], v[46:47], v[8:9]\n"
    "v_pk_mul_f32 v[64:65], v[64:65], v[10:11]\n"
    "v_pk_mul_f32 v[66:67], v[66:67], v[12:13]\n"
    "v_pk_mul_f32 v[48:49], v[48:49], v[6:7]\n"
    "v_pk_mul_f32 v[50:51], v[50:51], v[8:9]\n"
    "v_pk_mul_f32 v[68:69], v[68:69], v[10:11]\n"
    "v_pk_mul_f32 v[70:71], v[70:71], v[12:13]\n"
    "v_pk_mul_f32 v[52:53], v[52:53], v[6:7]\n"
    "v_pk_mul_f32 v[54:55], v[54:55], v[8:9]\n"
    "v_pk_mul_f32 v[72:73], v[72:73], v[10:11]\n"
    "v_pk_mul_f32 v[74:75], v[74:75], v[12:13]\n"
    "v_lshlrev_b32 v11, 4, v1\n"
    "s_lshl_b32 s92, s68, 2\n"
    "v_add_u32 v11, 156416, v11\n"
    "v_lshlrev_b32 v13, 3, v1\n"
    "v_add_u32 v12, s92, v11\n"
    "s_waitcnt lgkmcnt(14)\n"
    "v_fma_f32 v96, v14, v36, v37\n"
    "s_waitcnt lgkmcnt(14)\n"
    "v_fma_f32 v97, v16, v40, v41\n"
    "s_waitcnt lgkmcnt(14)\n"
    "v_fma_f32 v98, v18, v44, v45\n"
    "s_waitcnt lgkmcnt(14)\n"
    "v_fma_f32 v99, v20, v48, v49\n"
    "s_waitcnt lgkmcnt(14)\n"
    "v_fma_f32 v100, v22, v52, v53\n"
    "v_fmac_f32_dpp v96, -v36, v15 quad_perm:[1,0,3,2] row_mask:0xf bank_mask:0xf\n"
    "v_fmac_f32_dpp v97, -v40, v17 quad_perm:[1,0,3,2] row_mask:0xf bank_mask:0xf\n"
    "v_fmac_f32_dpp v98, -v44, v19 quad_perm:[1,0,3,2] row_mask:0xf bank_mask:0xf\n"
    "v_fmac_f32_dpp v99, -v48, v21 quad_perm:[1,0,3,2] row_mask:0xf bank_mask:0xf\n"
    "v_fmac_f32_dpp v100, -v52, v23 quad_perm:[1,0,3,2] row_mask:0xf bank_mask:0xf\n"
    "v_fma_f32 v101, v14, v96, v38\n"
    "v_fma_f32 v102, v16, v97, v42\n"
    "v_fma_f32 v103, v18, v98, v46\n"
    "v_fma_f32 v104, v20, v99, v50\n"
    "v_fma_f32 v105, v22, v100, v54\n"
    "v_fmac_f32_dpp v101, -v96, v15 quad_perm:[1,0,3,2] row_mask:0xf bank_mask:0xf\n"
    "v_fmac_f32_dpp v102, -v97, v17 quad_perm:[1,0,3,2] row_mask:0xf bank_mask:0xf\n"
    "v_fmac_f32_dpp v103, -v98, v19 quad_perm:[1,0,3,2] row_mask:0xf bank_mask:0xf\n"
    "v_fmac_f32_dpp v104, -v99, v21 quad_perm:[1,0,3,2] row_mask:0xf bank_mask:0xf\n"
    "v_fmac_f32_dpp v105, -v100, v23 quad_perm:[1,0,3,2] row_mask:0xf bank_mask:0xf\n"
    "v_fma_f32 v96, v14, v101, v39\n"
    "v_fma_f32 v97, v16, v102, v43\n"
    "v_fma_f32 v98, v18, v103, v47\n"
    "v_fma_f32 v99, v20, v104, v51\n"
    "v_fma_f32 v100, v22, v105, v55\n"
    "v_fmac_f32_dpp v96, -v101, v15 quad_perm:[1,0,3,2] row_mask:0xf bank_mask:0xf\n"
    "v_fmac_f32_dpp v97, -v102, v17 quad_perm:[1,0,3,2] row_mask:0xf bank_mask:0xf\n"
    "v_fmac_f32_dpp v98, -v103, v19 quad_perm:[1,0,3,2] row_mask:0xf bank_mask:0xf\n"
    "v_fmac_f32_dpp v99, -v104, v21 quad_perm:[1,0,3,2] row_mask:0xf bank_mask:0xf\n"
    "v_fmac_f32_dpp v100, -v105, v23 quad_perm:[1,0,3,2] row_mask:0xf bank_mask:0xf\n"
    "v_fma_f32 v101, v14, v96, v56\n"
    "v_fma_f32 v102, v16, v97, v60\n"
    "v_fma_f32 v103, v18, v98, v64\n"
    "v_fma_f32 v104, v20, v99, v68\n"
    "v_fma_f32 v105, v22, v100, v72\n"
    "v_fmac_f32_dpp v101, -v96, v15 quad_perm:[1,0,3,2] row_mask:0xf bank_mask:0xf\n"
    "v_fmac_f32_dpp v102, -v97, v17 quad_perm:[1,0,3,2] row_mask:0xf bank_mask:0xf\n"
    "v_fmac_f32_dpp v103, -v98, v19 quad_perm:[1,0,3,2] row_mask:0xf bank_mask:0xf\n"
    "v_fmac_f32_dpp v104, -v99, v21 quad_perm:[1,0,3,2] row_mask:0xf bank_mask:0xf\n"
    "v_fmac_f32_dpp v105, -v100, v23 quad_perm:[1,0,3,2] row_mask:0xf bank_mask:0xf\n"
    "v_fma_f32 v96, v14, v101, v57\n"
    "v_fma_f32 v97, v16, v102, v61\n"
    "v_fma_f32 v98, v18, v103, v65\n"
    "v_fma_f32 v99, v20, v104, v69\n"
    "v_fma_f32 v100, v22, v105, v73\n"
    "v_fmac_f32_dpp v96, -v101, v15 quad_perm:[1,0,3,2] row_mask:0xf bank_mask:0xf\n"
    "v_fmac_f32_dpp v97, -v102, v17 quad_perm:[1,0,3,2] row_mask:0xf bank_mask:0xf\n"
    "v_fmac_f32_dpp v98, -v103, v19 quad_perm:[1,0,3,2] row_mask:0xf bank_mask:0xf\n"
    "v_fmac_f32_dpp v99, -v104, v21 quad_perm:[1,0,3,2] row_mask:0xf bank_mask:0xf\n"
    "v_fmac_f32_dpp v100, -v105, v23 quad_perm:[1,0,3,2] row_mask:0xf bank_mask:0xf\n"
    "v_fma_f32 v101, v14, v96, v58\n"
    "v_fma_f32 v102, v16, v97, v62\n"
    "v_fma_f32 v103, v18, v98, v66\n"
    "v_fma_f32 v104, v20, v99, v70\n"
    "v_fma_f32 v105, v22, v100, v74\n"
    "v_fmac_f32_dpp v101, -v96, v15 quad_perm:[1,0,3,2] row_mask:0xf bank_mask:0xf\n"
    "v_fmac_f32_dpp v102, -v97, v17 quad_perm:[1,0,3,2] row_mask:0xf bank_mask:0xf\n"
    "v_fmac_f32_dpp v103, -v98, v19 quad_perm:[1,0,3,2] row_mask:0xf bank_mask:0xf\n"
    "v_fmac_f32_dpp v104, -v99, v21 quad_perm:[1,0,3,2] row_mask:0xf bank_mask:0xf\n"
    "v_fmac_f32_dpp v105, -v100, v23 quad_perm:[1,0,3,2] row_mask:0xf bank_mask:0xf\n"
    "v_fma_f32 v96, v14, v101, v59\n"
    "v_fma_f32 v97, v16, v102, v63\n"
    "v_fma_f32 v98, v18, v103, v67\n"
    "v_fma_f32 v99, v20, v104, v71\n"
    "v_fma_f32 v100, v22, v105, v75\n"
    "v_fmac_f32_dpp v96, -v101, v15 quad_perm:[1,0,3,2] row_mask:0xf bank_mask:0xf\n"
    "v_fmac_f32_dpp v97, -v102, v17 quad_perm:[1,0,3,2] row_mask:0xf bank_mask:0xf\n"
    "v_fmac_f32_dpp v98, -v103, v19 quad_perm:[1,0,3,2] row_mask:0xf bank_mask:0xf\n"
    "v_fmac_f32_dpp v99, -v104, v21 quad_perm:[1,0,3,2] row_mask:0xf bank_mask:0xf\n"
    "v_fmac_f32_dpp v100, -v105, v23 quad_perm:[1,0,3,2] row_mask:0xf bank_mask:0xf\n"
    "s_waitcnt lgkmcnt(14)\n"
    "v_mul_f32 v24, v122, v96\n"
    "s_waitcnt lgkmcnt(14)\n"
    "v_mul_f32 v25, v124, v97\n"
    "s_waitcnt lgkmcnt(14)\n"
    "v_mul_f32 v26, v126, v98\n"
    "s_waitcnt lgkmcnt(14)\n"
    "v_mul_f32 v27, v128, v99\n"
    "s_waitcnt lgkmcnt(14)\n"
    "v_mul_f32 v28, v130, v100\n"
    "v_fmac_f32_dpp v24, v96, v123 quad_perm:[1,0,3,2] row_mask:0xf bank_mask:0xf\n"
    "v_fmac_f32_dpp v25, v97, v125 quad_perm:[1,0,3,2] row_mask:0xf bank_mask:0xf\n"
    "v_fmac_f32_dpp v26, v98, v127 quad_perm:[1,0,3,2] row_mask:0xf bank_mask:0xf\n"
    "v_fmac_f32_dpp v27, v99, v129 quad_perm:[1,0,3,2] row_mask:0xf bank_mask:0xf\n"
    "v_fmac_f32_dpp v28, v100, v131 quad_perm:[1,0,3,2] row_mask:0xf bank_mask:0xf\n"
    "v_mov_b32 v6, v24\n"
    "v_mov_b32 v7, v25\n"
    "v_mov_b32 v8, v26\n"
    "v_mov_b32 v9, v27\n"
    "v_mov_b32 v10, v28\n"
    "v_permlane16_swap_b32 v24, v6\n"
    "v_permlane16_swap_b32 v25, v7\n"
    "v_permlane16_swap_b32 v26, v8\n"
    "v_permlane16_swap_b32 v27, v9\n"
    "v_permlane16_swap_b32 v28, v10\n"
    "s_waitcnt lgkmcnt(14)\n"
    "v_fmac_f32 v6, v112, v24\n"
    "s_waitcnt lgkmcnt(13)\n"
    "v_fmac_f32 v7, v114, v25\n"
    "s_waitcnt lgkmcnt(12)\n"
    "v_fmac_f32 v8, v116, v26\n"
    "s_waitcnt lgkmcnt(11)\n"
    "v_fmac_f32 v9, v118, v27\n"
    "s_waitcnt lgkmcnt(10)\n"
    "v_fmac_f32 v10, v120, v28\n"
    "v_fmac_f32_dpp v6, v24, v113 quad_perm:[1,0,3,2] row_mask:0xf bank_mask:0xf\n"
    "v_fmac_f32_dpp v7, v25, v115 quad_perm:[1,0,3,2] row_mask:0xf bank_mask:0xf\n"
    "v_fmac_f32_dpp v8, v26, v117 quad_perm:[1,0,3,2] row_mask:0xf bank_mask:0xf\n"
    "v_fmac_f32_dpp v9, v27, v119 quad_perm:[1,0,3,2] row_mask:0xf bank_mask:0xf\n"
    "v_fmac_f32_dpp v10, v28, v121 quad_perm:[1,0,3,2] row_mask:0xf bank_mask:0xf\n"
    "v_mov_b32 v29, v6\n"
    "v_mov_b32 v30, v7\n"
    "v_mov_b32 v31, v8\n"
    "v_mov_b32 v32, v9\n"
    "v_mov_b32 v33, v10\n"
    "v_permlane32_swap_b32 v29, v6\n"
    "v_permlane32_swap_b32 v30, v7\n"
    "v_permlane32_swap_b32 v31, v8\n"
    "v_permlane32_swap_b32 v32, v9\n"
    "v_permlane32_swap_b32 v33, v10\n"
    "s_waitcnt lgkmcnt(9)\n"
    "v_fmac_f32 v6, v76, v29\n"
    "s_waitcnt lgkmcnt(8)\n"
    "v_fmac_f32 v7, v78, v30\n"
    "s_waitcnt lgkmcnt(7)\n"
    "v_fmac_f32 v8, v80, v31\n"
    "s_waitcnt lgkmcnt(6)\n"
    "v_fmac_f32 v9, v82, v32\n"
    "s_waitcnt lgkmcnt(5)\n"
    "v_fmac_f32 v10, v84, v33\n"
    "v_fmac_f32_dpp v6, v29, v77 quad_perm:[1,0,3,2] row_mask:0xf bank_mask:0xf\n"
    "v_fmac_f32_dpp v7, v30, v79 quad_perm:[1,0,3,2] row_mask:0xf bank_mask:0xf\n"
    "v_fmac_f32_dpp v8, v31, v81 quad_perm:[1,0,3,2] row_mask:0xf bank_mask:0xf\n"
    "v_fmac_f32_dpp v9, v32, v83 quad_perm:[1,0,3,2] row_mask:0xf bank_mask:0xf\n"
    "v_fmac_f32_dpp v10, v33, v85 quad_perm:[1,0,3,2] row_mask:0xf bank_mask:0xf\n"
    "s_mov_b64 exec, 0xffff\n"
    "ds_write_b32 v12, v6 offset:0\n"
    "ds_write_b32 v12, v7 offset:256\n"
    "ds_write_b32 v12, v8 offset:512\n"
    "ds_write_b32 v12, v9 offset:768\n"
    "ds_write_b32 v12, v10 offset:1024\n"
    "s_mov_b64 exec, 1\n"
    "v_mov_b32 v34, 161024\n"
    "v_mov_b32 v35, 1\n"
    "ds_add_u32 v34, v35\n"
    "s_mov_b64 exec, -1\n"
    "v_mul_u32_u24 v34, 640, v2\n"
    "s_mul_i32 s92, s68, 640\n"
    "v_add_u32 v34, v34, v5\n"
    "v_add_u32 v35, s92, v5\n"
    "ds_read_b64 v[112:113], v34 offset:0\n"
    "ds_read_b64 v[114:115], v34 offset:128\n"
    "ds_read_b64 v[116:117], v34 offset:256\n"
    "ds_read_b64 v[118:119], v34 offset:384\n"
    "ds_read_b64 v[120:121], v34 offset:512\n"
    "ds_read_b64 v[76:77], v35 offset:2560\n"
    "ds_read_b64 v[78:79], v35 offset:2688\n"
    "ds_read_b64 v[80:81], v35 offset:2816\n"
    "ds_read_b64 v[82:83], v35 offset:2944\n"
    "ds_read_b64 v[84:85], v35 offset:3072\n"
    "v_mov_b32 v34, 161024\n"
    "s_mov_b32 s0, 524288\n"
    "s_waitcnt lgkmcnt(0)\n"
    "Lk_lw_16:\n"
    "ds_read_b32 v35, v34\n"
    "s_waitcnt lgkmcnt(0)\n"
    "v_readfirstlane_b32 s92, v35\n"
    "s_nop 1\n"
    "s_cmp_ge_u32 s92, 4\n"
    "s_cbranch_scc1 Lk_lwd_17\n"
    "s_sub_u32 s0, s0, 1\n"
    "s_cbranch_scc0 Lk_lw_16\n"
    "Lk_lwd_17:\n"
    "ds_read_b128 v[96:99], v11 offset:0\n"
    "ds_read_b128 v[100:103], v11 offset:256\n"
    "ds_read_b128 v[104:107], v11 offset:512\n"
    "ds_read_b128 v[108:111], v11 offset:768\n"
    "ds_read_b128 v[132:135], v11 offset:1024\n"
    "s_waitcnt lgkmcnt(4)\n"
    "v_fmac_f32 v97, v86, v96\n"
    "s_waitcnt lgkmcnt(3)\n"
    "v_fmac_f32 v101, v88, v100\n"
    "s_waitcnt lgkmcnt(2)\n"
    "v_fmac_f32 v105, v90, v104\n"
    "s_waitcnt lgkmcnt(1)\n"
    "v_fmac_f32 v109, v92, v108\n"
    "s_waitcnt lgkmcnt(0)\n"
    "v_fmac_f32 v133, v94, v132\n"
    "v_fmac_f32_dpp v97, v96, v87 quad_perm:[1,0,3,2] row_mask:0xf bank_mask:0xf\n"
    "v_fmac_f32_dpp v101, v100, v89 quad_perm:[1,0,3,2] row_mask:0xf bank_mask:0xf\n"
    "v_fmac_f32_dpp v105, v104, v91 quad_perm:[1,0,3,2] row_mask:0xf bank_mask:0xf\n"
    "v_fmac_f32_dpp v109, v108, v93 quad_perm:[1,0,3,2] row_mask:0xf bank_mask:0xf\n"
    "v_fmac_f32_dpp v133, v132, v95 quad_perm:[1,0,3,2] row_mask:0xf bank_mask:0xf\n"
    "v_fmac_f32 v98, v86, v97\n"
    "v_fmac_f32 v102, v88, v101\n"
    "v_fmac_f32 v106, v90, v105\n"
    "v_fmac_f32 v110, v92, v109\n"
    "v_fmac_f32 v134, v94, v133\n"
    "v_fmac_f32_dpp v98, v97, v87 quad_perm:[1,0,3,2] row_mask:0xf bank_mask:0xf\n"
    "v_fmac_f32_dpp v102, v101, v89 quad_perm:[1,0,3,2] row_mask:0xf bank_mask:0xf\n"
    "v_fmac_f32_dpp v106, v105, v91 quad_perm:[1,0,3,2] row_mask:0xf bank_mask:0xf\n"
    "v_fmac_f32_dpp v110, v109, v93 quad_perm:[1,0,3,2] row_mask:0xf bank_mask:0xf\n"
    "v_fmac_f32_dpp v134, v133, v95 quad_perm:[1,0,3,2] row_mask:0xf bank_mask:0xf\n"
    "v_fmac_f32 v99, v86, v98\n"
    "v_fmac_f32 v103, v88, v102\n"
    "v_fmac_f32 v107, v90, v106\n"
    "v_fmac_f32 v111, v92, v110\n"
    "v_fmac_f32 v135, v94, v134\n"
    "v_fmac_f32_dpp v99, v98, v87 quad_perm:[1,0,3,2] row_mask:0xf bank_mask:0xf\n"
    "v_fmac_f32_dpp v103, v102, v89 quad_perm:[1,0,3,2] row_mask:0xf bank_mask:0xf\n"
    "v_fmac_f32_dpp v107, v106, v91 quad_perm:[1,0,3,2] row_mask:0xf bank_mask:0xf\n"
    "v_fmac_f32_dpp v111, v110, v93 quad_perm:[1,0,3,2] row_mask:0xf bank_mask:0xf\n"
    "v_fmac_f32_dpp v135, v134, v95 quad_perm:[1,0,3,2] row_mask:0xf bank_mask:0xf\n"
    "s_cmp_eq_u32 s68, 0\n"
    "s_cbranch_scc0 Lk_skip_18\n"
    "s_mov_b64 exec, 0xffff\n"
    "v_mov_b32 v98, v99\n"
    "v_mov_b32 v99, s73\n"
    "buffer_store_dwordx2 v[98:99], v13, s[24:27], s29 offen offset:0 sc1\n"
    "v_mov_b32 v102, v103\n"
    "v_mov_b32 v103, s73\n"
    "buffer_store_dwordx2 v[102:103], v13, s[24:27], s29 offen offset:128 sc1\n"
    "v_mov_b32 v106, v107\n"
    "v_mov_b32 v107, s73\n"
    "buffer_store_dwordx2 v[106:107], v13, s[24:27], s29 offen offset:256 sc1\n"
    "v_mov_b32 v110, v111\n"
    "v_mov_b32 v111, s73\n"
    "buffer_store_dwordx2 v[110:111], v13, s[24:27], s29 offen offset:384 sc1\n"
    "v_mov_b32 v134, v135\n"
    "v_mov_b32 v135, s73\n"
    "buffer_store_dwordx2 v[134:135], v13, s[24:27], s29 offen offset:512 sc1\n"
    "s_mov_b64 exec, -1\n"
    "Lk_skip_18:\n"
    "v_subrev_u32 v34, 2, v2\n"
    "v_cmp_gt_u32 vcc, 2, v2\n"
    "s_nop 1\n"
    "v_cndmask_b32 v34, v34, 23, vcc\n"
    "v_mul_u32_u24 v34, 640, v34\n"
    "v_add_u32 v34, v34, v5\n"
    "ds_read_b64 v[86:87], v34 offset:0\n"
    "ds_read_b64 v[88:89], v34 offset:128\n"
    "ds_read_b64 v[90:91], v34 offset:256\n"
    "ds_read_b64 v[92:93], v34 offset:384\n"
    "ds_read_b64 v[94:95], v34 offset:512\n"
    "v_cndmask_b32 v6, 0, v24, s[88:89]\n"
    "v_cndmask_b32 v7, 0, v25, s[88:89]\n"
    "v_cndmask_b32 v8, 0, v26, s[88:89]\n"
    "v_cndmask_b32 v9, 0, v27, s[88:89]\n"
    "v_cndmask_b32 v10, 0, v28, s[88:89]\n"
    "v_cndmask_b32 v24, 0, v96, s[34:35]\n"
    "v_cndmask_b32 v25, 0, v100, s[34:35]\n"
    "v_cndmask_b32 v26, 0, v104, s[34:35]\n"
    "v_cndmask_b32 v27, 0, v108, s[34:35]\n"
    "v_cndmask_b32 v28, 0, v132, s[34:35]\n"
    "v_cndmask_b32 v24, v24, v97, s[36:37]\n"
    "v_cndmask_b32 v25, v25, v101, s[36:37]\n"
    "v_cndmask_b32 v26, v26, v105, s[36:37]\n"
    "v_cndmask_b32 v27, v27, v109, s[36:37]\n"
    "v_cndmask_b32 v28, v28, v133, s[36:37]\n"
    "v_cndmask_b32 v24, v24, v98, s[38:39]\n"
    "v_cndmask_b32 v25, v25, v102, s[38:39]\n"
    "v_cndmask_b32 v26, v26, v106, s[38:39]\n"
    "v_cndmask_b32 v27, v27, v110, s[38:39]\n"
    "v_cndmask_b32 v28, v28, v134, s[38:39]\n"
    "s_waitcnt lgkmcnt(4)\n"
    "v_fmac_f32 v6, v86, v29\n"
    "s_waitcnt lgkmcnt(3)\n"
    "v_fmac_f32 v7, v88, v30\n"
    "s_waitcnt lgkmcnt(2)\n"
    "v_fmac_f32 v8, v90, v31\n"
    "s_waitcnt lgkmcnt(1)\n"
    "v_fmac_f32 v9, v92, v32\n"
    "s_waitcnt lgkmcnt(0)\n"
    "v_fmac_f32 v10, v94, v33\n"
    "v_fmac_f32_dpp v6, v29, v87 quad_perm:[1,0,3,2] row_mask:0xf bank_mask:0xf\n"
    "v_fmac_f32_dpp v7, v30, v89 quad_perm:[1,0,3,2] row_mask:0xf bank_mask:0xf\n"
    "v_fmac_f32_dpp v8, v31, v91 quad_perm:[1,0,3,2] row_mask:0xf bank_mask:0xf\n"
    "v_fmac_f32_dpp v9, v32, v93 quad_perm:[1,0,3,2] row_mask:0xf bank_mask:0xf\n"
    "v_fmac_f32_dpp v10, v33, v95 quad_perm:[1,0,3,2] row_mask:0xf bank_mask:0xf\n"
    "v_fmac_f32 v6, v112, v24\n"
    "v_fmac_f32 v7, v114, v25\n"
    "v_fmac_f32 v8, v116, v26\n"
    "v_fmac_f32 v9, v118, v27\n"
    "v_fmac_f32 v10, v120, v28\n"
    "v_fmac_f32_dpp v6, v24, v113 quad_perm:[1,0,3,2] row_mask:0xf bank_mask:0xf\n"
    "v_fmac_f32_dpp v7, v25, v115 quad_perm:[1,0,3,2] row_mask:0xf bank_mask:0xf\n"
    "v_fmac_f32_dpp v8, v26, v117 quad_perm:[1,0,3,2] row_mask:0xf bank_mask:0xf\n"
    "v_fmac_f32_dpp v9, v27, v119 quad_perm:[1,0,3,2] row_mask:0xf bank_mask:0xf\n"
    "v_fmac_f32_dpp v10, v28, v121 quad_perm:[1,0,3,2] row_mask:0xf bank_mask:0xf\n"
    "v_mul_f32 v86, v112, v76\n"
    "v_mul_f32 v88, v114, v78\n"
    "v_mul_f32 v90, v116, v80\n"
    "v_mul_f32 v92, v118, v82\n"
    "v_mul_f32 v94, v120, v84\n"
    "v_mul_f32 v87, v112, v77\n"
    "v_mul_f32 v89, v114, v79\n"
    "v_mul_f32 v91, v116, v81\n"
    "v_mul_f32 v93, v118, v83\n"
    "v_mul_f32 v95, v120, v85\n"
    "v_fma_f32 v86, -v113, v77, v86\n"
    "v_fma_f32 v88, -v115, v79, v88\n"
    "v_fma_f32 v90, -v117, v81, v90\n"
    "v_fma_f32 v92, -v119, v83, v92\n"
    "v_fma_f32 v94, -v121, v85, v94\n"
    "v_fmac_f32 v87, v113, v76\n"
    "v_fmac_f32 v89, v115, v78\n"
    "v_fmac_f32 v91, v117, v80\n"
    "v_fmac_f32 v93, v119, v82\n"
    "v_fmac_f32 v95, v121, v84\n"
    "s_lshl_b32 s92, s68, 2\n"
    "v_add_u32 v35, s92, v2\n"
    "v_mul_u32_u24 v34, 640, v35\n"
    "v_add_u32 v34, v34, v5\n"
    "ds_read_b64 v[112:113], v34 offset:5120\n"
    "ds_read_b64 v[114:115], v34 offset:5248\n"
    "ds_read_b64 v[116:117], v34 offset:5376\n"
    "ds_read_b64 v[118:119], v34 offset:5504\n"
    "ds_read_b64 v[120:121], v34 offset:5632\n"
    "v_cmp_gt_u32 s[90:91], s69, v35\n"
    "v_mul_u32_u24 v35, 10240, v35\n"
    "v_add_u32 v35, v35, v13\n"
    "v_mul_f32_dpp v24, v36, v123 quad_perm:[1,0,3,2] row_mask:0xf bank_mask:0xf\n"
    "v_mul_f32_dpp v25, v40, v125 quad_perm:[1,0,3,2] row_mask:0xf bank_mask:0xf\n"
    "v_mul_f32_dpp v26, v44, v127 quad_perm:[1,0,3,2] row_mask:0xf bank_mask:0xf\n"
    "v_mul_f32_dpp v27, v48, v129 quad_perm:[1,0,3,2] row_mask:0xf bank_mask:0xf\n"
    "v_mul_f32_dpp v28, v52, v131 quad_perm:[1,0,3,2] row_mask:0xf bank_mask:0xf\n"
    "buffer_load_dwordx4 v[212:215], v3, s[24:27], s55 offen offset:0 sc1\n"
    "v_fma_f32 v36, v122, v36, v24\n"
    "v_fma_f32 v40, v124, v40, v25\n"
    "v_fma_f32 v44, v126, v44, v26\n"
    "v_fma_f32 v48, v128, v48, v27\n"
    "v_fma_f32 v52, v130, v52, v28\n"
    "buffer_load_dwordx4 v[216:219], v3, s[24:27], s55 offen offset:1024 sc1\n"
    "v_mul_f32_dpp v24, v37, v123 quad_perm:[1,0,3,2] row_mask:0xf bank_mask:0xf\n"
    "v_mul_f32_dpp v25, v41, v125 quad_perm:[1,0,3,2] row_mask:0xf bank_mask:0xf\n"
    "v_mul_f32_dpp v26, v45, v127 quad_perm:[1,0,3,2] row_mask:0xf bank_mask:0xf\n"
    "v_mul_f32_dpp v27, v49, v129 quad_perm:[1,0,3,2] row_mask:0xf bank_mask:0xf\n"
    "v_mul_f32_dpp v28, v53, v131 quad_perm:[1,0,3,2] row_mask:0xf bank_mask:0xf\n"
    "buffer_load_dwordx4 v[220:223], v3, s[24:27], s55 offen offset:2048 sc1\n"
    "v_fma_f32 v37, v122, v37, v24\n"
    "v_fma_f32 v41, v124, v41, v25\n"
    "v_fma_f32 v45, v126, v45, v26\n"
    "v_fma_f32 v49, v128, v49, v27\n"
    "v_fma_f32 v53, v130, v53, v28\n"
    "buffer_load_dwordx4 v[224:227], v3, s[24:27], s55 offen offset:3072 sc1\n"
    "v_mul_f32_dpp v24, v38, v123 quad_perm:[1,0,3,2] row_mask:0xf bank_mask:0xf\n"
    "v_mul_f32_dpp v25, v42, v125 quad_perm:[1,0,3,2] row_mask:0xf bank_mask:0xf\n"
    "v_mul_f32_dpp v26, v46, v127 quad_perm:[1,0,3,2] row_mask:0xf bank_mask:0xf\n"
    "v_mul_f32_dpp v27, v50, v129 quad_perm:[1,0,3,2] row_mask:0xf bank_mask:0xf\n"
    "v_mul_f32_dpp v28, v54, v131 quad_perm:[1,0,3,2] row_mask:0xf bank_mask:0xf\n"
    "s_add_u32 s95, s55, 4096\n"
    "buffer_load_dwordx4 v[228:231], v3, s[24:27], s95 offen sc1\n"
    "v_fma_f32 v38, v122, v38, v24\n"
    "v_fma_f32 v42, v124, v42, v25\n"
    "v_fma_f32 v46, v126, v46, v26\n"
    "v_fma_f32 v50, v128, v50, v27\n"
    "v_fma_f32 v54, v130, v54, v28\n"
    "buffer_load_dwordx4 v[232:235], v3, s[24:27], s56 offen offset:0 sc1\n"
    "v_mul_f32_dpp v24, v39, v123 quad_perm:[1,0,3,2] row_mask:0xf bank_mask:0xf\n"
    "v_mul_f32_dpp v25, v43, v125 quad_perm:[1,0,3,2] row_mask:0xf bank_mask:0xf\n"
    "v_mul_f32_dpp v26, v47, v127 quad_perm:[1,0,3,2] row_mask:0xf bank_mask:0xf\n"
    "v_mul_f32_dpp v27, v51, v129 quad_perm:[1,0,3,2] row_mask:0xf bank_mask:0xf\n"
    "v_mul_f32_dpp v28, v55, v131 quad_perm:[1,0,3,2] row_mask:0xf bank_mask:0xf\n"
    "buffer_load_dwordx4 v[236:239], v3, s[24:27], s56 offen offset:1024 sc1\n"
    "v_fma_f32 v39, v122, v39, v24\n"
    "v_fma_f32 v43, v124, v43, v25\n"
    "v_fma_f32 v47, v126, v47, v26\n"
    "v_fma_f32 v51, v128, v51, v27\n"
    "v_fma_f32 v55, v130, v55, v28\n"
    "buffer_load_dwordx4 v[240:243], v3, s[24:27], s56 offen offset:2048 sc1\n"
    "v_mul_f32_dpp v24, v56, v123 quad_perm:[1,0,3,2] row_mask:0xf bank_mask:0xf\n"
    "v_mul_f32_dpp v25, v60, v125 quad_perm:[1,0,3,2] row_mask:0xf bank_mask:0xf\n"
    "v_mul_f32_dpp v26, v64, v127 quad_perm:[1,0,3,2] row_mask:0xf bank_mask:0xf\n"
    "v_mul_f32_dpp v27, v68, v129 quad_perm:[1,0,3,2] row_mask:0xf bank_mask:0xf\n"
    "v_mul_f32_dpp v28, v72, v131 quad_perm:[1,0,3,2] row_mask:0xf bank_mask:0xf\n"
    "buffer_load_dwordx4 v[244:247], v3, s[24:27], s56 offen offset:3072 sc1\n"
    "v_fma_f32 v56, v122, v56, v24\n"
    "v_fma_f32 v60, v124, v60, v25\n"
    "v_fma_f32 v64, v126, v64, v26\n"
    "v_fma_f32 v68, v128, v68, v27\n"
    "v_fma_f32 v72, v130, v72, v28\n"
    "s_add_u32 s95, s56, 4096\n"
    "buffer_load_dwordx4 v[248:251], v3, s[24:27], s95 offen sc1\n"
    "v_mul_f32_dpp v24, v57, v123 quad_perm:[1,0,3,2] row_mask:0xf bank_mask:0xf\n"
    "v_mul_f32_dpp v25, v61, v125 quad_perm:[1,0,3,2] row_mask:0xf bank_mask:0xf\n"
    "v_mul_f32_dpp v26, v65, v127 quad_perm:[1,0,3,2] row_mask:0xf bank_mask:0xf\n"
    "v_mul_f32_dpp v27, v69, v129 quad_perm:[1,0,3,2] row_mask:0xf bank_mask:0xf\n"
    "v_mul_f32_dpp v28, v73, v131 quad_perm:[1,0,3,2] row_mask:0xf bank_mask:0xf\n"
    "v_fma_f32 v57, v122, v57, v24\n"
    "v_fma_f32 v61, v124, v61, v25\n"
    "v_fma_f32 v65, v126, v65, v26\n"
    "v_fma_f32 v69, v128, v69, v27\n"
    "v_fma_f32 v73, v130, v73, v28\n"
    "v_mul_f32_dpp v24, v58, v123 quad_perm:[1,0,3,2] row_mask:0xf bank_mask:0xf\n"
    "v_mul_f32_dpp v25, v62, v125 quad_perm:[1,0,3,2] row_mask:0xf bank_mask:0xf\n"
    "v_mul_f32_dpp v26, v66, v127 quad_perm:[1,0,3,2] row_mask:0xf bank_mask:0xf\n"
    "v_mul_f32_dpp v27, v70, v129 quad_perm:[1,0,3,2] row_mask:0xf bank_mask:0xf\n"
    "v_mul_f32_dpp v28, v74, v131 quad_perm:[1,0,3,2] row_mask:0xf bank_mask:0xf\n"
    "v_fma_f32 v58, v122, v58, v24\n"
    "v_fma_f32 v62, v124, v62, v25\n"
    "v_fma_f32 v66, v126, v66, v26\n"
    "v_fma_f32 v70, v128, v70, v27\n"
    "v_fma_f32 v74, v130, v74, v28\n"
    "v_mul_f32_dpp v24, v59, v123 quad_perm:[1,0,3,2] row_mask:0xf bank_mask:0xf\n"
    "v_mul_f32_dpp v25, v63, v125 quad_perm:[1,0,3,2] row_mask:0xf bank_mask:0xf\n"
    "v_mul_f32_dpp v26, v67, v127 quad_perm:[1,0,3,2] row_mask:0xf bank_mask:0xf\n"
    "v_mul_f32_dpp v27, v71, v129 quad_perm:[1,0,3,2] row_mask:0xf bank_mask:0xf\n"
    "v_mul_f32_dpp v28, v75, v131 quad_perm:[1,0,3,2] row_mask:0xf bank_mask:0xf\n"
    "v_fma_f32 v59, v122, v59, v24\n"
    "v_fma_f32 v63, v124, v63, v25\n"
    "v_fma_f32 v67, v126, v67, v26\n"
    "v_fma_f32 v71, v128, v71, v27\n"
    "v_fma_f32 v75, v130, v75, v28\n"
    "s_mul_i32 s93, s68, 5760\n"
    "s_add_u32 s93, s93, 81920\n"
    "v_and_b32 v24, 1, v1\n"
    "v_lshrrev_b32 v25, 1, v1\n"
    "v_lshl_add_u32 v24, v24, 3, v25\n"
    "v_mul_u32_u24 v24, 72, v24\n"
    "v_lshl_add_u32 v24, v2, 3, v24\n"
    "v_add_u32 v81, s93, v24\n"
    "v_lshrrev_b32 v24, 1, v2\n"
    "v_lshrrev_b32 v25, 2, v1\n"
    "v_lshl_add_u32 v25, v24, 3, v25\n"
    "v_mul_u32_u24 v25, 72, v25\n"
    "v_and_b32 v26, 1, v2\n"
    "v_lshl_add_u32 v25, v26, 5, v25\n"
    "v_and_b32 v27, 3, v1\n"
    "v_lshl_add_u32 v25, v27, 3, v25\n"
    "v_add_u32 v82, s93, v25\n"
    "v_lshlrev_b32 v26, 10, v26\n"
    "v_lshl_add_u32 v26, v1, 4, v26\n"
    "s_mul_i32 s93, s70, 10\n"
    "v_add_u32 v24, s93, v24\n"
    "v_lshrrev_b32 v25, 2, v24\n"
    "v_lshl_add_u32 v25, v25, 12, v26\n"
    "v_and_b32 v27, 3, v24\n"
    "v_lshl_add_u32 v76, v27, 8, v25\n"
    "v_add_u32 v24, 2, v24\n"
    "v_lshrrev_b32 v25, 2, v24\n"
    "v_lshl_add_u32 v25, v25, 12, v26\n"
    "v_and_b32 v27, 3, v24\n"
    "v_lshl_add_u32 v77, v27, 8, v25\n"
    "v_add_u32 v24, 2, v24\n"
    "v_lshrrev_b32 v25, 2, v24\n"
    "v_lshl_add_u32 v25, v25, 12, v26\n"
    "v_and_b32 v27, 3, v24\n"
    "v_lshl_add_u32 v78, v27, 8, v25\n"
    "v_add_u32 v24, 2, v24\n"
    "v_lshrrev_b32 v25, 2, v24\n"
    "v_lshl_add_u32 v25, v25, 12, v26\n"
    "v_and_b32 v27, 3, v24\n"
    "v_lshl_add_u32 v79, v27, 8, v25\n"
    "v_add_u32 v24, 2, v24\n"
    "v_lshrrev_b32 v25, 2, v24\n"
    "v_lshl_add_u32 v25, v25, 12, v26\n"
    "v_and_b32 v27, 3, v24\n"
    "v_lshl_add_u32 v80, v27, 8, v25\n"
    "s_setprio 2\n"
    "v_mov_b32 v106, 0\n"
    "v_mov_b32 v107, 0\n"
    "v_mov_b32 v108, 0\n"
    "v_mov_b32 v109, 0\n"
    "v_mov_b32 v110, 0\n"
    "s_cmp_lt_u32 s92, s69\n"
    "s_cbranch_scc0 Lk_skip_19\n"
    "s_mov_b32 s0, 131072\n"
    "s_nop 4\n"
    "Lk_poll_20:\n"
    "buffer_load_dwordx2 v[96:97], v35, s[24:27], s30 offen offset:0 sc1\n"
    "buffer_load_dwordx2 v[98:99], v35, s[24:27], s30 offen offset:128 sc1\n"
    "buffer_load_dwordx2 v[100:101], v35, s[24:27], s30 offen offset:256 sc1\n"
    "buffer_load_dwordx2 v[102:103], v35, s[24:27], s30 offen offset:384 sc1\n"
    "buffer_load_dwordx2 v[104:105], v35, s[24:27], s30 offen offset:512 sc1\n"
    "Lk_chk_22:\n"
    "s_waitcnt vmcnt(0)\n"
    "v_xor_b32 v24, s73, v97\n"
    "v_xor_b32 v25, s73, v99\n"
    "v_xor_b32 v26, s73, v101\n"
    "v_xor_b32 v27, s73, v103\n"
    "v_xor_b32 v28, s73, v105\n"
    "v_or3_b32 v24, v24, v25, v26\n"
    "v_or3_b32 v24, v24, v27, v28\n"
    "v_cndmask_b32 v24, 0, v24, s[90:91]\n"
    "v_cmp_ne_u32 vcc, 0, v24\n"
    "s_nop 4\n"
    "s_cbranch_vccz Lk_done_21\n"
    "s_sleep 4\n"
    "s_sub_u32 s0, s0, 1\n"
    "s_cbranch_scc0 Lk_poll_20\n"
    "Lk_done_21:\n"
    "v_cndmask_b32 v96, 0, v96, s[90:91]\n"
    "v_cndmask_b32 v98, 0, v98, s[90:91]\n"
    "v_cndmask_b32 v100, 0, v100, s[90:91]\n"
    "v_cndmask_b32 v102, 0, v102, s[90:91]\n"
    "v_cndmask_b32 v104, 0, v104, s[90:91]\n"
    "s_waitcnt lgkmcnt(4)\n"
    "v_mul_f32 v106, v112, v96\n"
    "s_waitcnt lgkmcnt(3)\n"
    "v_mul_f32 v107, v114, v98\n"
    "s_waitcnt lgkmcnt(2)\n"
    "v_mul_f32 v108, v116, v100\n"
    "s_waitcnt lgkmcnt(1)\n"
    "v_mul_f32 v109, v118, v102\n"
    "s_waitcnt lgkmcnt(0)\n"
    "v_mul_f32 v110, v120, v104\n"
    "v_fmac_f32_dpp v106, v96, v113 quad_perm:[1,0,3,2] row_mask:0xf bank_mask:0xf\n"
    "v_fmac_f32_dpp v107, v98, v115 quad_perm:[1,0,3,2] row_mask:0xf bank_mask:0xf\n"
    "v_fmac_f32_dpp v108, v100, v117 quad_perm:[1,0,3,2] row_mask:0xf bank_mask:0xf\n"
    "v_fmac_f32_dpp v109, v102, v119 quad_perm:[1,0,3,2] row_mask:0xf bank_mask:0xf\n"
    "v_fmac_f32_dpp v110, v104, v121 quad_perm:[1,0,3,2] row_mask:0xf bank_mask:0xf\n"
    "v_mov_b32 v96, v106\n"
    "v_mov_b32 v97, v107\n"
    "v_mov_b32 v98, v108\n"
    "v_mov_b32 v99, v109\n"
    "v_mov_b32 v100, v110\n"
    "v_permlane16_swap_b32 v106, v96\n"
    "v_permlane16_swap_b32 v107, v97\n"
    "v_permlane16_swap_b32 v108, v98\n"
    "v_permlane16_swap_b32 v109, v99\n"
    "v_permlane16_swap_b32 v110, v100\n"
    "v_add_f32 v106, v106, v96\n"
    "v_add_f32 v107, v107, v97\n"
    "v_add_f32 v108, v108, v98\n"
    "v_add_f32 v109, v109, v99\n"
    "v_add_f32 v110, v110, v100\n"
    "v_mov_b32 v96, v106\n"
    "v_mov_b32 v97, v107\n"
    "v_mov_b32 v98, v108\n"
    "v_mov_b32 v99, v109\n"
    "v_mov_b32 v100, v110\n"
    "v_permlane32_swap_b32 v106, v96\n"
    "v_permlane32_swap_b32 v107, v97\n"
    "v_permlane32_swap_b32 v108, v98\n"
    "v_permlane32_swap_b32 v109, v99\n"
    "v_permlane32_swap_b32 v110, v100\n"
    "v_add_f32 v106, v106, v96\n"
    "v_add_f32 v107, v107, v97\n"
    "v_add_f32 v108, v108, v98\n"
    "v_add_f32 v109, v109, v99\n"
    "v_add_f32 v110, v110, v100\n"
    "Lk_skip_19:\n"
    "s_mov_b64 exec, 0xffff\n"
    "ds_write_b32 v12, v106 offset:1280\n"
    "ds_write_b32 v12, v107 offset:1536\n"
    "ds_write_b32 v12, v108 offset:1792\n"
    "ds_write_b32 v12, v109 offset:2048\n"
    "ds_write_b32 v12, v110 offset:2304\n"
    "s_mov_b64 exec, 1\n"
    "v_mov_b32 v34, 161028\n"
    "v_mov_b32 v35, 1\n"
    "ds_add_u32 v34, v35\n"
    "s_mov_b64 exec, -1\n"
    "v_mov_b32 v34, 161028\n"
    "s_mov_b32 s0, 524288\n"
    "s_waitcnt lgkmcnt(0)\n"
    "Lk_lw_23:\n"
    "ds_read_b32 v35, v34\n"
    "s_waitcnt lgkmcnt(0)\n"
    "v_readfirstlane_b32 s92, v35\n"
    "s_nop 1\n"
    "s_cmp_ge_u32 s92, 4\n"
    "s_cbranch_scc1 Lk_lwd_24\n"
    "s_sub_u32 s0, s0, 1\n"
    "s_cbranch_scc0 Lk_lw_23\n"
    "Lk_lwd_24:\n"
    "ds_read_b128 v[96:99], v11 offset:1280\n"
    "ds_read_b128 v[100:103], v11 offset:1536\n"
    "ds_read_b128 v[104:107], v11 offset:1792\n"
    "ds_read_b128 v[108:111], v11 offset:2048\n"
    "ds_read_b128 v[132:135], v11 offset:2304\n"
    "s_waitcnt lgkmcnt(4)\n"
    "v_pk_add_f32 v[96:97], v[96:97], v[98:99]\n"
    "s_waitcnt lgkmcnt(3)\n"
    "v_pk_add_f32 v[100:101], v[100:101], v[102:103]\n"
    "s_waitcnt lgkmcnt(2)\n"
    "v_pk_add_f32 v[104:105], v[104:105], v[106:107]\n"
    "s_waitcnt lgkmcnt(1)\n"
    "v_pk_add_f32 v[108:109], v[108:109], v[110:111]\n"
    "s_waitcnt lgkmcnt(0)\n"
    "v_pk_add_f32 v[132:133], v[132:133], v[134:135]\n"
    "v_add_f32 v96, v96, v97\n"
    "v_add_f32 v100, v100, v101\n"
    "v_add_f32 v104, v104, v105\n"
    "v_add_f32 v108, v108, v109\n"
    "v_add_f32 v132, v132, v133\n"
    "v_fmac_f32 v6, v86, v96\n"
    "v_fmac_f32 v7, v88, v100\n"
    "v_fmac_f32 v8, v90, v104\n"
    "v_fmac_f32 v9, v92, v108\n"
    "v_fmac_f32 v10, v94, v132\n"
    "v_fmac_f32_dpp v6, v96, v87 quad_perm:[1,0,3,2] row_mask:0xf bank_mask:0xf\n"
    "v_fmac_f32_dpp v7, v100, v89 quad_perm:[1,0,3,2] row_mask:0xf bank_mask:0xf\n"
    "v_fmac_f32_dpp v8, v104, v91 quad_perm:[1,0,3,2] row_mask:0xf bank_mask:0xf\n"
    "v_fmac_f32_dpp v9, v108, v93 quad_perm:[1,0,3,2] row_mask:0xf bank_mask:0xf\n"
    "v_fmac_f32_dpp v10, v132, v95 quad_perm:[1,0,3,2] row_mask:0xf bank_mask:0xf\n"
    "v_fmac_f32 v36, v14, v6\n"
    "v_fmac_f32 v40, v16, v7\n"
    "v_fmac_f32 v44, v18, v8\n"
    "v_fmac_f32 v48, v20, v9\n"
    "v_fmac_f32 v52, v22, v10\n"
    "v_fmac_f32_dpp v36, v6, v15 quad_perm:[1,0,3,2] row_mask:0xf bank_mask:0xf\n"
    "v_fmac_f32_dpp v40, v7, v17 quad_perm:[1,0,3,2] row_mask:0xf bank_mask:0xf\n"
    "v_fmac_f32_dpp v44, v8, v19 quad_perm:[1,0,3,2] row_mask:0xf bank_mask:0xf\n"
    "v_fmac_f32_dpp v48, v9, v21 quad_perm:[1,0,3,2] row_mask:0xf bank_mask:0xf\n"
    "v_fmac_f32_dpp v52, v10, v23 quad_perm:[1,0,3,2] row_mask:0xf bank_mask:0xf\n"
    "v_fmac_f32 v37, v14, v36\n"
    "v_fmac_f32 v41, v16, v40\n"
    "v_fmac_f32 v45, v18, v44\n"
    "v_fmac_f32 v49, v20, v48\n"
    "v_fmac_f32 v53, v22, v52\n"
    "v_fmac_f32_dpp v37, v36, v15 quad_perm:[1,0,3,2] row_mask:0xf bank_mask:0xf\n"
    "v_fmac_f32_dpp v41, v40, v17 quad_perm:[1,0,3,2] row_mask:0xf bank_mask:0xf\n"
    "v_fmac_f32_dpp v45, v44, v19 quad_perm:[1,0,3,2] row_mask:0xf bank_mask:0xf\n"
    "v_fmac_f32_dpp v49, v48, v21 quad_perm:[1,0,3,2] row_mask:0xf bank_mask:0xf\n"
    "v_fmac_f32_dpp v53, v52, v23 quad_perm:[1,0,3,2] row_mask:0xf bank_mask:0xf\n"
    "v_fmac_f32 v38, v14, v37\n"
    "v_fmac_f32 v42, v16, v41\n"
    "v_fmac_f32 v46, v18, v45\n"
    "v_fmac_f32 v50, v20, v49\n"
    "v_fmac_f32 v54, v22, v53\n"
    "v_fmac_f32_dpp v38, v37, v15 quad_perm:[1,0,3,2] row_mask:0xf bank_mask:0xf\n"
    "v_fmac_f32_dpp v42, v41, v17 quad_perm:[1,0,3,2] row_mask:0xf bank_mask:0xf\n"
    "v_fmac_f32_dpp v46, v45, v19 quad_perm:[1,0,3,2] row_mask:0xf bank_mask:0xf\n"
    "v_fmac_f32_dpp v50, v49, v21 quad_perm:[1,0,3,2] row_mask:0xf bank_mask:0xf\n"
    "v_fmac_f32_dpp v54, v53, v23 quad_perm:[1,0,3,2] row_mask:0xf bank_mask:0xf\n"
    "v_fmac_f32 v39, v14, v38\n"
    "v_fmac_f32 v43, v16, v42\n"
    "v_fmac_f32 v47, v18, v46\n"
    "v_fmac_f32 v51, v20, v50\n"
    "v_fmac_f32 v55, v22, v54\n"
    "v_fmac_f32_dpp v39, v38, v15 quad_perm:[1,0,3,2] row_mask:0xf bank_mask:0xf\n"
    "v_fmac_f32_dpp v43, v42, v17 quad_perm:[1,0,3,2] row_mask:0xf bank_mask:0xf\n"
    "v_fmac_f32_dpp v47, v46, v19 quad_perm:[1,0,3,2] row_mask:0xf bank_mask:0xf\n"
    "v_fmac_f32_dpp v51, v50, v21 quad_perm:[1,0,3,2] row_mask:0xf bank_mask:0xf\n"
    "v_fmac_f32_dpp v55, v54, v23 quad_perm:[1,0,3,2] row_mask:0xf bank_mask:0xf\n"
    "v_cvt_pk_bf16_f32 v96, v36, v37\n"
    "v_cvt_pk_bf16_f32 v97, v38, v39\n"
    "ds_write_b64 v81, v[96:97] offset:0\n"
    "v_cvt_pk_bf16_f32 v98, v40, v41\n"
    "v_cvt_pk_bf16_f32 v99, v42, v43\n"
    "ds_write_b64 v81, v[98:99] offset:1152\n"
    "v_cvt_pk_bf16_f32 v100, v44, v45\n"
    "v_cvt_pk_bf16_f32 v101, v46, v47\n"
    "ds_write_b64 v81, v[100:101] offset:2304\n"
    "v_cvt_pk_bf16_f32 v102, v48, v49\n"
    "v_cvt_pk_bf16_f32 v103, v50, v51\n"
    "ds_write_b64 v81, v[102:103] offset:3456\n"
    "v_cvt_pk_bf16_f32 v104, v52, v53\n"
    "v_cvt_pk_bf16_f32 v105, v54, v55\n"
    "ds_write_b64 v81, v[104:105] offset:4608\n"
    "v_fmac_f32 v56, v14, v39\n"
    "v_fmac_f32 v60, v16, v43\n"
    "v_fmac_f32 v64, v18, v47\n"
    "v_fmac_f32 v68, v20, v51\n"
    "v_fmac_f32 v72, v22, v55\n"
    "v_fmac_f32_dpp v56, v39, v15 quad_perm:[1,0,3,2] row_mask:0xf bank_mask:0xf\n"
    "v_fmac_f32_dpp v60, v43, v17 quad_perm:[1,0,3,2] row_mask:0xf bank_mask:0xf\n"
    "v_fmac_f32_dpp v64, v47, v19 quad_perm:[1,0,3,2] row_mask:0xf bank_mask:0xf\n"
    "v_fmac_f32_dpp v68, v51, v21 quad_perm:[1,0,3,2] row_mask:0xf bank_mask:0xf\n"
    "v_fmac_f32_dpp v72, v55, v23 quad_perm:[1,0,3,2] row_mask:0xf bank_mask:0xf\n"
    "v_fmac_f32 v57, v14, v56\n"
    "v_fmac_f32 v61, v16, v60\n"
    "v_fmac_f32 v65, v18, v64\n"
    "v_fmac_f32 v69, v20, v68\n"
    "v_fmac_f32 v73, v22, v72\n"
    "v_fmac_f32_dpp v57, v56, v15 quad_perm:[1,0,3,2] row_mask:0xf bank_mask:0xf\n"
    "v_fmac_f32_dpp v61, v60, v17 quad_perm:[1,0,3,2] row_mask:0xf bank_mask:0xf\n"
    "v_fmac_f32_dpp v65, v64, v19 quad_perm:[1,0,3,2] row_mask:0xf bank_mask:0xf\n"
    "v_fmac_f32_dpp v69, v68, v21 quad_perm:[1,0,3,2] row_mask:0xf bank_mask:0xf\n"
    "v_fmac_f32_dpp v73, v72, v23 quad_perm:[1,0,3,2] row_mask:0xf bank_mask:0xf\n"
    "v_fmac_f32 v58, v14, v57\n"
    "v_fmac_f32 v62, v16, v61\n"
    "v_fmac_f32 v66, v18, v65\n"
    "v_fmac_f32 v70, v20, v69\n"
    "v_fmac_f32 v74, v22, v73\n"
    "v_fmac_f32_dpp v58, v57, v15 quad_perm:[1,0,3,2] row_mask:0xf bank_mask:0xf\n"
    "v_fmac_f32_dpp v62, v61, v17 quad_perm:[1,0,3,2] row_mask:0xf bank_mask:0xf\n"
    "v_fmac_f32_dpp v66, v65, v19 quad_perm:[1,0,3,2] row_mask:0xf bank_mask:0xf\n"
    "v_fmac_f32_dpp v70, v69, v21 quad_perm:[1,0,3,2] row_mask:0xf bank_mask:0xf\n"
    "v_fmac_f32_dpp v74, v73, v23 quad_perm:[1,0,3,2] row_mask:0xf bank_mask:0xf\n"
    "v_fmac_f32 v59, v14, v58\n"
    "v_fmac_f32 v63, v16, v62\n"
    "v_fmac_f32 v67, v18, v66\n"
    "v_fmac_f32 v71, v20, v70\n"
    "v_fmac_f32 v75, v22, v74\n"
    "v_fmac_f32_dpp v59, v58, v15 quad_perm:[1,0,3,2] row_mask:0xf bank_mask:0xf\n"
    "v_fmac_f32_dpp v63, v62, v17 quad_perm:[1,0,3,2] row_mask:0xf bank_mask:0xf\n"
    "v_fmac_f32_dpp v67, v66, v19 quad_perm:[1,0,3,2] row_mask:0xf bank_mask:0xf\n"
    "v_fmac_f32_dpp v71, v70, v21 quad_perm:[1,0,3,2] row_mask:0xf bank_mask:0xf\n"
    "v_fmac_f32_dpp v75, v74, v23 quad_perm:[1,0,3,2] row_mask:0xf bank_mask:0xf\n"
    "v_cvt_pk_bf16_f32 v106, v56, v57\n"
    "v_cvt_pk_bf16_f32 v107, v58, v59\n"
    "ds_write_b64 v81, v[106:107] offset:32\n"
    "v_cvt_pk_bf16_f32 v108, v60, v61\n"
    "v_cvt_pk_bf16_f32 v109, v62, v63\n"
    "ds_write_b64 v81, v[108:109] offset:1184\n"
    "v_cvt_pk_bf16_f32 v110, v64, v65\n"
    "v_cvt_pk_bf16_f32 v111, v66, v67\n"
    "ds_write_b64 v81, v[110:111] offset:2336\n"
    "v_cvt_pk_bf16_f32 v112, v68, v69\n"
    "v_cvt_pk_bf16_f32 v113, v70, v71\n"
    "ds_write_b64 v81, v[112:113] offset:3488\n"
    "v_cvt_pk_bf16_f32 v114, v72, v73\n"
    "v_cvt_pk_bf16_f32 v115, v74, v75\n"
    "ds_write_b64 v81, v[114:115] offset:4640\n"
    "ds_read_b64_tr_b16 v[116:117], v82 offset:0\n"
    "ds_read_b64_tr_b16 v[118:119], v82 offset:288\n"
    "ds_read_b64_tr_b16 v[120:121], v82 offset:1152\n"
    "ds_read_b64_tr_b16 v[122:123], v82 offset:1440\n"
    "ds_read_b64_tr_b16 v[124:125], v82 offset:2304\n"
    "ds_read_b64_tr_b16 v[126:127], v82 offset:2592\n"
    "ds_read_b64_tr_b16 v[128:129], v82 offset:3456\n"
    "ds_read_b64_tr_b16 v[130:131], v82 offset:3744\n"
    "ds_read_b64_tr_b16 v[132:133], v82 offset:4608\n"
    "ds_read_b64_tr_b16 v[134:135], v82 offset:4896\n"
    "s_waitcnt lgkmcnt(8)\n"
    "buffer_store_dwordx4 v[116:119], v76, s[24:27], s28 offen sc1\n"
    "s_waitcnt lgkmcnt(6)\n"
    "buffer_store_dwordx4 v[120:123], v77, s[24:27], s28 offen sc1\n"
    "s_waitcnt lgkmcnt(4)\n"
    "buffer_store_dwordx4 v[124:127], v78, s[24:27], s28 offen sc1\n"
    "s_waitcnt lgkmcnt(2)\n"
    "buffer_store_dwordx4 v[128:131], v79, s[24:27], s28 offen sc1\n"
    "s_waitcnt lgkmcnt(0)\n"
    "buffer_store_dwordx4 v[132:135], v80, s[24:27], s28 offen sc1\n"
    "s_mov_b64 exec, 1\n"
    "v_mov_b32 v34, 161032\n"
    "v_mov_b32 v35, 1\n"
    "ds_add_u32 v34, v35\n"
    "s_mov_b64 exec, -1\n"
    "s_add_u32 s92, s68, 0\n"
    "s_and_b32 s92, s92, 3\n"
    "s_lshl_b32 s92, s92, 10\n"
    "v_add_u32 v5, s92, v3\n"
    "s_add_u32 s92, s68, 1\n"
    "s_and_b32 s92, s92, 3\n"
    "s_lshl_b32 s92, s92, 10\n"
    "v_add_u32 v6, s92, v3\n"
    "s_add_u32 s92, s68, 2\n"
    "s_and_b32 s92, s92, 3\n"
    "s_lshl_b32 s92, s92, 10\n"
    "v_add_u32 v7, s92, v3\n"
    "s_add_u32 s92, s68, 3\n"
    "s_and_b32 s92, s92, 3\n"
    "s_lshl_b32 s92, s92, 10\n"
    "v_add_u32 v8, s92, v3\n"
    "v_bfe_u32 v15, v0, 2, 1\n"
    "v_and_b32 v14, 3, v0\n"
    "v_lshl_add_u32 v14, v15, 4, v14\n"
    "v_lshlrev_b32 v14, 2, v14\n"
    "v_add_u32 v29, 0, v0\n"
    "v_mul_u32_u24 v30, 3277, v29\n"
    "v_lshrrev_b32 v30, 16, v30\n"
    "v_mul_u32_u24 v31, 20, v30\n"
    "v_sub_u32 v29, v29, v31\n"
    "v_lshlrev_b32 v29, 4, v29\n"
    "v_mul_u32_u24 v31, 336, v30\n"
    "v_add3_u32 v9, v31, v29, s33\n"
    "v_lshrrev_b32 v31, 2, v30\n"
    "v_and_b32 v30, 3, v30\n"
    "v_lshl_add_u32 v30, v31, 3, v30\n"
    "v_mul_u32_u24 v30, 2560, v30\n"
    "v_add_u32 v24, v30, v29\n"
    "v_add_u32 v29, 64, v0\n"
    "v_mul_u32_u24 v30, 3277, v29\n"
    "v_lshrrev_b32 v30, 16, v30\n"
    "v_mul_u32_u24 v31, 20, v30\n"
    "v_sub_u32 v29, v29, v31\n"
    "v_lshlrev_b32 v29, 4, v29\n"
    "v_mul_u32_u24 v31, 336, v30\n"
    "v_add3_u32 v10, v31, v29, s33\n"
    "v_lshrrev_b32 v31, 2, v30\n"
    "v_and_b32 v30, 3, v30\n"
    "v_lshl_add_u32 v30, v31, 3, v30\n"
    "v_mul_u32_u24 v30, 2560, v30\n"
    "v_add_u32 v25, v30, v29\n"
    "v_add_u32 v29, 128, v0\n"
    "v_mul_u32_u24 v30, 3277, v29\n"
    "v_lshrrev_b32 v30, 16, v30\n"
    "v_mul_u32_u24 v31, 20, v30\n"
    "v_sub_u32 v29, v29, v31\n"
    "v_lshlrev_b32 v29, 4, v29\n"
    "v_mul_u32_u24 v31, 336, v30\n"
    "v_add3_u32 v11, v31, v29, s33\n"
    "v_lshrrev_b32 v31, 2, v30\n"
    "v_and_b32 v30, 3, v30\n"
    "v_lshl_add_u32 v30, v31, 3, v30\n"
    "v_mul_u32_u24 v30, 2560, v30\n"
    "v_add_u32 v26, v30, v29\n"
    "v_add_u32 v29, 192, v0\n"
    "v_mul_u32_u24 v30, 3277, v29\n"
    "v_lshrrev_b32 v30, 16, v30\n"
    "v_mul_u32_u24 v31, 20, v30\n"
    "v_sub_u32 v29, v29, v31\n"
    "v_lshlrev_b32 v29, 4, v29\n"
    "v_mul_u32_u24 v31, 336, v30\n"
    "v_add3_u32 v12, v31, v29, s33\n"
    "v_lshrrev_b32 v31, 2, v30\n"
    "v_and_b32 v30, 3, v30\n"
    "v_lshl_add_u32 v30, v31, 3, v30\n"
    "v_mul_u32_u24 v30, 2560, v30\n"
    "v_add_u32 v27, v30, v29\n"
    "v_add_u32 v29, 256, v0\n"
    "v_mul_u32_u24 v30, 3277, v29\n"
    "v_lshrrev_b32 v30, 16, v30\n"
    "v_mul_u32_u24 v31, 20, v30\n"
    "v_sub_u32 v29, v29, v31\n"
    "v_lshlrev_b32 v29, 4, v29\n"
    "v_mul_u32_u24 v31, 336, v30\n"
    "v_add3_u32 v13, v31, v29, s33\n"
    "v_lshrrev_b32 v31, 2, v30\n"
    "v_and_b32 v30, 3, v30\n"
    "v_lshl_add_u32 v30, v31, 3, v30\n"
    "v_mul_u32_u24 v30, 2560, v30\n"
    "v_add_u32 v28, v30, v29\n"
    "s_waitcnt vmcnt(0)\n"
    "s_mov_b64 exec, 1\n"
    "v_mov_b32 v30, s73\n"
    "v_mov_b32 v31, 0\n"
    "buffer_store_dword v30, v31, s[24:27], s98 offen sc1\n"
    "s_mov_b64 exec, -1\n"
    "v_mov_b32 v116, v24\n"
    "v_mov_b32 v117, v25\n"
    "v_mov_b32 v118, v26\n"
    "v_mov_b32 v119, v27\n"
    "v_mov_b32 v120, v28\n"
    "s_add_u32 s92, s68, 1\n"
    "s_and_b32 s92, s92, 3\n"
    "s_mul_i32 s92, s92, 15360\n"
    "s_add_u32 s92, s92, 81920\n"
    "v_add_u32 v121, s92, v3\n"
    "s_add_u32 s92, s68, 2\n"
    "s_and_b32 s92, s92, 3\n"
    "s_mul_i32 s92, s92, 15360\n"
    "s_add_u32 s92, s92, 87040\n"
    "v_add_u32 v122, s92, v3\n"
    "s_add_u32 s92, s68, 3\n"
    "s_and_b32 s92, s92, 3\n"
    "s_mul_i32 s92, s92, 15360\n"
    "s_add_u32 s92, s92, 92160\n"
    "v_add_u32 v123, s92, v3\n"
    "s_mov_b32 s0, 131072\n"
    "s_nop 4\n"
    "Lk_poll_25:\n"
    "buffer_load_dword v15, v14, s[24:27], s99 offen sc1\n"
    "Lk_chk_27:\n"
    "s_waitcnt vmcnt(0)\n"
    "v_cmp_ne_u32 vcc, s73, v15\n"
    "s_nop 4\n"
    "s_cbranch_vccz Lk_done_26\n"
    "s_sleep 4\n"
    "s_sub_u32 s0, s0, 1\n"
    "s_cbranch_scc0 Lk_poll_25\n"
    "Lk_done_26:\n"
    "s_setprio 0\n"
    "buffer_load_dwordx4 v[16:19], v5, s[24:27], s40 offen sc1\n"
    "buffer_load_dwordx4 v[20:23], v6, s[24:27], s40 offen sc1\n"
    "buffer_load_dwordx4 v[24:27], v7, s[24:27], s40 offen sc1\n"
    "buffer_load_dwordx4 v[28:31], v8, s[24:27], s40 offen sc1\n"
    "buffer_load_dwordx4 v[32:35], v5, s[24:27], s41 offen sc1\n"
    "buffer_load_dwordx4 v[124:127], v6, s[24:27], s41 offen sc1\n"
    "buffer_load_dwordx4 v[128:131], v7, s[24:27], s41 offen sc1\n"
    "buffer_load_dwordx4 v[132:135], v8, s[24:27], s41 offen sc1\n"
    "buffer_load_dword v15, v14, s[24:27], s59 offen sc1\n"
    "s_waitcnt vmcnt(8)\n"
    "v_mfma_f32_16x16x32_bf16 v[36:39], v[152:155], v[16:19], 0\n"
    "v_mfma_f32_16x16x32_bf16 v[40:43], v[156:159], v[16:19], 0\n"
    "v_mfma_f32_16x16x32_bf16 v[44:47], v[160:163], v[16:19], 0\n"
    "v_mfma_f32_16x16x32_bf16 v[48:51], v[164:167], v[16:19], 0\n"
    "v_mfma_f32_16x16x32_bf16 v[52:55], v[168:171], v[16:19], 0\n"
    "s_waitcnt vmcnt(7)\n"
    "v_mfma_f32_16x16x32_bf16 v[56:59], v[152:155], v[20:23], 0\n"
    "v_mfma_f32_16x16x32_bf16 v[60:63], v[156:159], v[20:23], 0\n"
    "v_mfma_f32_16x16x32_bf16 v[64:67], v[160:163], v[20:23], 0\n"
    "v_mfma_f32_16x16x32_bf16 v[68:71], v[164:167], v[20:23], 0\n"
    "v_mfma_f32_16x16x32_bf16 v[72:75], v[168:171], v[20:23], 0\n"
    "s_waitcnt vmcnt(6)\n"
    "v_mfma_f32_16x16x32_bf16 v[76:79], v[152:155], v[24:27], 0\n"
    "v_mfma_f32_16x16x32_bf16 v[80:83], v[156:159], v[24:27], 0\n"
    "v_mfma_f32_16x16x32_bf16 v[84:87], v[160:163], v[24:27], 0\n"
    "v_mfma_f32_16x16x32_bf16 v[88:91], v[164:167], v[24:27], 0\n"
    "v_mfma_f32_16x16x32_bf16 v[92:95], v[168:171], v[24:27], 0\n"
    "s_waitcnt vmcnt(5)\n"
    "v_mfma_f32_16x16x32_bf16 v[96:99], v[152:155], v[28:31], 0\n"
    "v_mfma_f32_16x16x32_bf16 v[100:103], v[156:159], v[28:31], 0\n"
    "v_mfma_f32_16x16x32_bf16 v[104:107], v[160:163], v[28:31], 0\n"
    "v_mfma_f32_16x16x32_bf16 v[108:111], v[164:167], v[28:31], 0\n"
    "v_mfma_f32_16x16x32_bf16 v[112:115], v[168:171], v[28:31], 0\n"
    "s_waitcnt vmcnt(4)\n"
    "v_mfma_f32_16x16x32_bf16 v[36:39], v[172:175], v[32:35], v[36:39]\n"
    "v_mfma_f32_16x16x32_bf16 v[40:43], v[176:179], v[32:35], v[40:43]\n"
    "v_mfma_f32_16x16x32_bf16 v[44:47], v[180:183], v[32:35], v[44:47]\n"
    "v_mfma_f32_16x16x32_bf16 v[48:51], v[184:187], v[32:35], v[48:51]\n"
    "v_mfma_f32_16x16x32_bf16 v[52:55], v[188:191], v[32:35], v[52:55]\n"
    "s_waitcnt vmcnt(3)\n"
    "v_mfma_f32_16x16x32_bf16 v[56:59], v[172:175], v[124:127], v[56:59]\n"
    "v_mfma_f32_16x16x32_bf16 v[60:63], v[176:179], v[124:127], v[60:63]\n"
    "v_mfma_f32_16x16x32_bf16 v[64:67], v[180:183], v[124:127], v[64:67]\n"
    "v_mfma_f32_16x16x32_bf16 v[68:71], v[184:187], v[124:127], v[68:71]\n"
    "v_mfma_f32_16x16x32_bf16 v[72:75], v[188:191], v[124:127], v[72:75]\n"
    "s_waitcnt vmcnt(0)\n"
    "v_cmp_ne_u32 vcc, s73, v15\n"
    "s_nop 4\n"
    "s_cbranch_vccz Lk_skip_28\n"
    "s_mov_b32 s0, 131072\n"
    "s_nop 4\n"
    "Lk_poll_29:\n"
    "buffer_load_dword v15, v14, s[24:27], s59 offen sc1\n"
    "Lk_chk_31:\n"
    "s_waitcnt vmcnt(0)\n"
    "v_cmp_ne_u32 vcc, s73, v15\n"
    "s_nop 4\n"
    "s_cbranch_vccz Lk_done_30\n"
    "s_sleep 4\n"
    "s_sub_u32 s0, s0, 1\n"
    "s_cbranch_scc0 Lk_poll_29\n"
    "Lk_done_30:\n"
    "Lk_skip_28:\n"
    "buffer_load_dwordx4 v[252:255], v5, s[24:27], s42 offen sc1\n"
    "buffer_load_dwordx4 v[136:139], v6, s[24:27], s42 offen sc1\n"
    "buffer_load_dwordx4 v[140:143], v7, s[24:27], s42 offen sc1\n"
    "buffer_load_dwordx4 v[144:147], v8, s[24:27], s42 offen sc1\n"
    "buffer_load_dwordx4 v[148:151], v6, s[24:27], s43 offen sc1\n"
    "buffer_load_dwordx4 v[16:19], v6, s[24:27], s44 offen sc1\n"
    "buffer_load_dwordx4 v[20:23], v7, s[24:27], s43 offen sc1\n"
    "buffer_load_dwordx4 v[24:27], v7, s[24:27], s44 offen sc1\n"
    "buffer_load_dwordx4 v[28:31], v8, s[24:27], s43 offen sc1\n"
    "buffer_load_dwordx4 v[32:35], v8, s[24:27], s44 offen sc1\n"
    "v_mfma_f32_16x16x32_bf16 v[76:79], v[172:175], v[128:131], v[76:79]\n"
    "v_mfma_f32_16x16x32_bf16 v[80:83], v[176:179], v[128:131], v[80:83]\n"
    "v_mfma_f32_16x16x32_bf16 v[84:87], v[180:183], v[128:131], v[84:87]\n"
    "v_mfma_f32_16x16x32_bf16 v[88:91], v[184:187], v[128:131], v[88:91]\n"
    "v_mfma_f32_16x16x32_bf16 v[92:95], v[188:191], v[128:131], v[92:95]\n"
    "buffer_load_dwordx4 v[124:127], v5, s[24:27], s43 offen sc1\n"
    "v_mfma_f32_16x16x32_bf16 v[96:99], v[172:175], v[132:135], v[96:99]\n"
    "v_mfma_f32_16x16x32_bf16 v[100:103], v[176:179], v[132:135], v[100:103]\n"
    "v_mfma_f32_16x16x32_bf16 v[104:107], v[180:183], v[132:135], v[104:107]\n"
    "v_mfma_f32_16x16x32_bf16 v[108:111], v[184:187], v[132:135], v[108:111]\n"
    "v_mfma_f32_16x16x32_bf16 v[112:115], v[188:191], v[132:135], v[112:115]\n"
    "buffer_load_dwordx4 v[128:131], v5, s[24:27], s44 offen sc1\n"
    "v_mov_b32 v14, 161032\n"
    "s_mov_b32 s0, 524288\n"
    "s_waitcnt lgkmcnt(0)\n"
    "Lk_lw_32:\n"
    "ds_read_b32 v15, v14\n"
    "s_waitcnt lgkmcnt(0)\n"
    "v_readfirstlane_b32 s92, v15\n"
    "s_nop 1\n"
    "s_cmp_ge_u32 s92, 4\n"
    "s_cbranch_scc1 Lk_lwd_33\n"
    "s_sub_u32 s0, s0, 1\n"
    "s_cbranch_scc0 Lk_lw_32\n"
    "Lk_lwd_33:\n"
    "s_waitcnt vmcnt(11)\n"
    "v_mfma_f32_16x16x32_bf16 v[36:39], v[192:195], v[252:255], v[36:39]\n"
    "v_mfma_f32_16x16x32_bf16 v[40:43], v[196:199], v[252:255], v[40:43]\n"
    "v_mfma_f32_16x16x32_bf16 v[44:47], v[200:203], v[252:255], v[44:47]\n"
    "v_mfma_f32_16x16x32_bf16 v[48:51], v[204:207], v[252:255], v[48:51]\n"
    "v_mfma_f32_16x16x32_bf16 v[52:55], v[208:211], v[252:255], v[52:55]\n"
    "buffer_load_dwordx4 v[132:135], v5, s[24:27], s45 offen sc1\n"
    "s_waitcnt vmcnt(11)\n"
    "v_mfma_f32_16x16x32_bf16 v[56:59], v[192:195], v[136:139], v[56:59]\n"
    "v_mfma_f32_16x16x32_bf16 v[60:63], v[196:199], v[136:139], v[60:63]\n"
    "v_mfma_f32_16x16x32_bf16 v[64:67], v[200:203], v[136:139], v[64:67]\n"
    "v_mfma_f32_16x16x32_bf16 v[68:71], v[204:207], v[136:139], v[68:71]\n"
    "v_mfma_f32_16x16x32_bf16 v[72:75], v[208:211], v[136:139], v[72:75]\n"
    "buffer_load_dwordx4 v[252:255], v6, s[24:27], s45 offen sc1\n"
    "s_waitcnt vmcnt(11)\n"
    "v_mfma_f32_16x16x32_bf16 v[76:79], v[192:195], v[140:143], v[76:79]\n"
    "v_mfma_f32_16x16x32_bf16 v[80:83], v[196:199], v[140:143], v[80:83]\n"
    "v_mfma_f32_16x16x32_bf16 v[84:87], v[200:203], v[140:143], v[84:87]\n"
    "v_mfma_f32_16x16x32_bf16 v[88:91], v[204:207], v[140:143], v[88:91]\n"
    "v_mfma_f32_16x16x32_bf16 v[92:95], v[208:211], v[140:143], v[92:95]\n"
    "buffer_load_dwordx4 v[136:139], v7, s[24:27], s45 offen sc1\n"
    "s_waitcnt vmcnt(11)\n"
    "v_mfma_f32_16x16x32_bf16 v[96:99], v[192:195], v[144:147], v[96:99]\n"
    "v_mfma_f32_16x16x32_bf16 v[100:103], v[196:199], v[144:147], v[100:103]\n"
    "v_mfma_f32_16x16x32_bf16 v[104:107], v[200:203], v[144:147], v[104:107]\n"
    "v_mfma_f32_16x16x32_bf16 v[108:111], v[204:207], v[144:147], v[108:111]\n"
    "v_mfma_f32_16x16x32_bf16 v[112:115], v[208:211], v[144:147], v[112:115]\n"
    "buffer_load_dwordx4 v[140:143], v8, s[24:27], s45 offen sc1\n"
    "s_waitcnt vmcnt(11)\n"
    "v_mfma_f32_16x16x32_bf16 v[56:59], v[212:215], v[148:151], v[56:59]\n"
    "v_mfma_f32_16x16x32_bf16 v[60:63], v[216:219], v[148:151], v[60:63]\n"
    "v_mfma_f32_16x16x32_bf16 v[64:67], v[220:223], v[148:151], v[64:67]\n"
    "v_mfma_f32_16x16x32_bf16 v[68:71], v[224:227], v[148:151], v[68:71]\n"
    "v_mfma_f32_16x16x32_bf16 v[72:75], v[228:231], v[148:151], v[72:75]\n"
    "buffer_load_dwordx4 v[144:147], v5, s[24:27], s46 offen sc1\n"
    "s_waitcnt vmcnt(11)\n"
    "v_mfma_f32_16x16x32_bf16 v[56:59], v[232:235], v[16:19], v[56:59]\n"
    "v_mfma_f32_16x16x32_bf16 v[60:63], v[236:239], v[16:19], v[60:63]\n"
    "v_mfma_f32_16x16x32_bf16 v[64:67], v[240:243], v[16:19], v[64:67]\n"
    "v_mfma_f32_16x16x32_bf16 v[68:71], v[244:247], v[16:19], v[68:71]\n"
    "v_mfma_f32_16x16x32_bf16 v[72:75], v[248:251], v[16:19], v[72:75]\n"
    "buffer_load_dwordx4 v[148:151], v6, s[24:27], s46 offen sc1\n"
    "s_waitcnt vmcnt(11)\n"
    "v_mfma_f32_16x16x32_bf16 v[76:79], v[212:215], v[20:23], v[76:79]\n"
    "v_mfma_f32_16x16x32_bf16 v[80:83], v[216:219], v[20:23], v[80:83]\n"
    "ds_write_b128 v121, v[56:59] offset:0\n"
    "v_mfma_f32_16x16x32_bf16 v[84:87], v[220:223], v[20:23], v[84:87]\n"
    "ds_write_b128 v121, v[60:63] offset:1024\n"
    "v_mfma_f32_16x16x32_bf16 v[88:91], v[224:227], v[20:23], v[88:91]\n"
    "ds_write_b128 v121, v[64:67] offset:2048\n"
    "v_mfma_f32_16x16x32_bf16 v[92:95], v[228:231], v[20:23], v[92:95]\n"
    "ds_write_b128 v121, v[68:71] offset:3072\n"
    "buffer_load_dwordx4 v[16:19], v7, s[24:27], s46 offen sc1\n"
    "s_waitcnt vmcnt(11)\n"
    "v_mfma_f32_16x16x32_bf16 v[76:79], v[232:235], v[24:27], v[76:79]\n"
    "v_mfma_f32_16x16x32_bf16 v[80:83], v[236:239], v[24:27], v[80:83]\n"
    "v_mfma_f32_16x16x32_bf16 v[84:87], v[240:243], v[24:27], v[84:87]\n"
    "v_mfma_f32_16x16x32_bf16 v[88:91], v[244:247], v[24:27], v[88:91]\n"
    "v_mfma_f32_16x16x32_bf16 v[92:95], v[248:251], v[24:27], v[92:95]\n"
    "ds_write_b128 v121, v[72:75] offset:4096\n"
    "buffer_load_dwordx4 v[20:23], v8, s[24:27], s46 offen sc1\n"
    "s_waitcnt vmcnt(11)\n"
    "v_mfma_f32_16x16x32_bf16 v[96:99], v[212:215], v[28:31], v[96:99]\n"
    "v_mfma_f32_16x16x32_bf16 v[100:103], v[216:219], v[28:31], v[100:103]\n"
    "ds_write_b128 v122, v[76:79] offset:0\n"
    "v_mfma_f32_16x16x32_bf16 v[104:107], v[220:223], v[28:31], v[104:107]\n"
    "ds_write_b128 v122, v[80:83] offset:1024\n"
    "v_mfma_f32_16x16x32_bf16 v[108:111], v[224:227], v[28:31], v[108:111]\n"
    "ds_write_b128 v122, v[84:87] offset:2048\n"
    "v_mfma_f32_16x16x32_bf16 v[112:115], v[228:231], v[28:31], v[112:115]\n"
    "ds_write_b128 v122, v[88:91] offset:3072\n"
    "buffer_load_dwordx4 v[24:27], v5, s[24:27], s47 offen sc1\n"
    "s_waitcnt vmcnt(11)\n"
    "v_mfma_f32_16x16x32_bf16 v[96:99], v[232:235], v[32:35], v[96:99]\n"
    "v_mfma_f32_16x16x32_bf16 v[100:103], v[236:239], v[32:35], v[100:103]\n"
    "v_mfma_f32_16x16x32_bf16 v[104:107], v[240:243], v[32:35], v[104:107]\n"
    "v_mfma_f32_16x16x32_bf16 v[108:111], v[244:247], v[32:35], v[108:111]\n"
    "v_mfma_f32_16x16x32_bf16 v[112:115], v[248:251], v[32:35], v[112:115]\n"
    "ds_write_b128 v122, v[92:95] offset:4096\n"
    "buffer_load_dwordx4 v[28:31], v6, s[24:27], s47 offen sc1\n"
    "s_waitcnt vmcnt(11)\n"
    "v_mfma_f32_16x16x32_bf16 v[36:39], v[212:215], v[124:127], v[36:39]\n"
    "ds_write_b128 v123, v[96:99] offset:0\n"
    "v_mfma_f32_16x16x32_bf16 v[40:43], v[216:219], v[124:127], v[40:43]\n"
    "ds_write_b128 v123, v[100:103] offset:1024\n"
    "v_mfma_f32_16x16x32_bf16 v[44:47], v[220:223], v[124:127], v[44:47]\n"
    "ds_write_b128 v123, v[104:107] offset:2048\n"
    "v_mfma_f32_16x16x32_bf16 v[48:51], v[224:227], v[124:127], v[48:51]\n"
    "ds_write_b128 v123, v[108:111] offset:3072\n"
    "v_mfma_f32_16x16x32_bf16 v[52:55], v[228:231], v[124:127], v[52:55]\n"
    "ds_write_b128 v123, v[112:115] offset:4096\n"
    "buffer_load_dwordx4 v[32:35], v7, s[24:27], s47 offen sc1\n"
    "s_waitcnt vmcnt(11)\n"
    "v_mfma_f32_16x16x32_bf16 v[36:39], v[232:235], v[128:131], v[36:39]\n"
    "v_mfma_f32_16x16x32_bf16 v[40:43], v[236:239], v[128:131], v[40:43]\n"
    "v_mfma_f32_16x16x32_bf16 v[44:47], v[240:243], v[128:131], v[44:47]\n"
    "v_mfma_f32_16x16x32_bf16 v[48:51], v[244:247], v[128:131], v[48:51]\n"
    "v_mfma_f32_16x16x32_bf16 v[52:55], v[248:251], v[128:131], v[52:55]\n"
    "buffer_load_dwordx4 v[124:127], v8, s[24:27], s47 offen sc1\n"
    "s_setprio 2\n"
    "s_mov_b64 exec, 1\n"
    "v_mov_b32 v14, 161040\n"
    "v_mov_b32 v15, 1\n"
    "ds_add_u32 v14, v15\n"
    "s_mov_b64 exec, -1\n"
    "v_mov_b32 v14, 161040\n"
    "s_mov_b32 s0, 524288\n"
    "s_waitcnt lgkmcnt(0)\n"
    "Lk_lw_34:\n"
    "ds_read_b32 v15, v14\n"
    "s_waitcnt lgkmcnt(0)\n"
    "v_readfirstlane_b32 s92, v15\n"
    "s_nop 1\n"
    "s_cmp_ge_u32 s92, 8\n"
    "s_cbranch_scc1 Lk_lwd_35\n"
    "s_sub_u32 s0, s0, 1\n"
    "s_cbranch_scc0 Lk_lw_34\n"
    "Lk_lwd_35:\n"
    "s_mul_i32 s92, s68, 15360\n"
    "s_add_u32 s92, s92, 81920\n"
    "v_add_u32 v14, s92, v3\n"
    "ds_read_b128 v[56:59], v14 offset:0\n"
    "ds_read_b128 v[60:63], v14 offset:1024\n"
    "ds_read_b128 v[64:67], v14 offset:2048\n"
    "ds_read_b128 v[68:71], v14 offset:3072\n"
    "ds_read_b128 v[72:75], v14 offset:4096\n"
    "ds_read_b128 v[76:79], v14 offset:5120\n"
    "ds_read_b128 v[80:83], v14 offset:6144\n"
    "ds_read_b128 v[84:87], v14 offset:7168\n"
    "ds_read_b128 v[88:91], v14 offset:8192\n"
    "ds_read_b128 v[92:95], v14 offset:9216\n"
    "ds_read_b128 v[96:99], v14 offset:10240\n"
    "ds_read_b128 v[100:103], v14 offset:11264\n"
    "ds_read_b128 v[104:107], v14 offset:12288\n"
    "ds_read_b128 v[108:111], v14 offset:13312\n"
    "ds_read_b128 v[112:115], v14 offset:14336\n"
    "s_waitcnt lgkmcnt(14)\n"
    "v_add_f32 v36, v36, v56\n"
    "v_add_f32 v37, v37, v57\n"
    "v_add_f32 v38, v38, v58\n"
    "v_add_f32 v39, v39, v59\n"
    "s_waitcnt lgkmcnt(13)\n"
    "v_add_f32 v40, v40, v60\n"
    "v_add_f32 v41, v41, v61\n"
    "v_add_f32 v42, v42, v62\n"
    "v_add_f32 v43, v43, v63\n"
    "s_waitcnt lgkmcnt(12)\n"
    "v_add_f32 v44, v44, v64\n"
    "v_add_f32 v45, v45, v65\n"
    "v_add_f32 v46, v46, v66\n"
    "v_add_f32 v47, v47, v67\n"
    "s_waitcnt lgkmcnt(11)\n"
    "v_add_f32 v48, v48, v68\n"
    "v_add_f32 v49, v49, v69\n"
    "v_add_f32 v50, v50, v70\n"
    "v_add_f32 v51, v51, v71\n"
    "s_waitcnt lgkmcnt(10)\n"
    "v_add_f32 v52, v52, v72\n"
    "v_add_f32 v53, v53, v73\n"
    "v_add_f32 v54, v54, v74\n"
    "v_add_f32 v55, v55, v75\n"
    "s_mul_i32 s92, s68, 5120\n"
    "s_add_u32 s92, s92, 61440\n"
    "v_add_u32 v14, s92, v3\n"
    "ds_read_b128 v[56:59], v14 offset:0\n"
    "ds_read_b128 v[60:63], v14 offset:1024\n"
    "ds_read_b128 v[64:67], v14 offset:2048\n"
    "ds_read_b128 v[68:71], v14 offset:3072\n"
    "ds_read_b128 v[72:75], v14 offset:4096\n"
    "s_waitcnt lgkmcnt(14)\n"
    "v_add_f32 v36, v36, v76\n"
    "v_add_f32 v37, v37, v77\n"
    "v_add_f32 v38, v38, v78\n"
    "v_add_f32 v39, v39, v79\n"
    "s_waitcnt lgkmcnt(13)\n"
    "v_add_f32 v40, v40, v80\n"
    "v_add_f32 v41, v41, v81\n"
    "v_add_f32 v42, v42, v82\n"
    "v_add_f32 v43, v43, v83\n"
    "s_waitcnt lgkmcnt(12)\n"
    "v_add_f32 v44, v44, v84\n"
    "v_add_f32 v45, v45, v85\n"
    "v_add_f32 v46, v46, v86\n"
    "v_add_f32 v47, v47, v87\n"
    "s_waitcnt lgkmcnt(11)\n"
    "v_add_f32 v48, v48, v88\n"
    "v_add_f32 v49, v49, v89\n"
    "v_add_f32 v50, v50, v90\n"
    "v_add_f32 v51, v51, v91\n"
    "s_waitcnt lgkmcnt(10)\n"
    "v_add_f32 v52, v52, v92\n"
    "v_add_f32 v53, v53, v93\n"
    "v_add_f32 v54, v54, v94\n"
    "v_add_f32 v55, v55, v95\n"
    "s_waitcnt lgkmcnt(9)\n"
    "v_add_f32 v36, v36, v96\n"
    "v_add_f32 v37, v37, v97\n"
    "v_add_f32 v38, v38, v98\n"
    "v_add_f32 v39, v39, v99\n"
    "s_waitcnt lgkmcnt(8)\n"
    "v_add_f32 v40, v40, v100\n"
    "v_add_f32 v41, v41, v101\n"
    "v_add_f32 v42, v42, v102\n"
    "v_add_f32 v43, v43, v103\n"
    "s_waitcnt lgkmcnt(7)\n"
    "v_add_f32 v44, v44, v104\n"
    "v_add_f32 v45, v45, v105\n"
    "v_add_f32 v46, v46, v106\n"
    "v_add_f32 v47, v47, v107\n"
    "s_waitcnt lgkmcnt(6)\n"
    "v_add_f32 v48, v48, v108\n"
    "v_add_f32 v49, v49, v109\n"
    "v_add_f32 v50, v50, v110\n"
    "v_add_f32 v51, v51, v111\n"
    "s_waitcnt lgkmcnt(5)\n"
    "v_add_f32 v52, v52, v112\n"
    "v_add_f32 v53, v53, v113\n"
    "v_add_f32 v54, v54, v114\n"
    "v_add_f32 v55, v55, v115\n"
    "s_waitcnt lgkmcnt(4)\n"
    "v_add_f32 v36, v36, v56\n"
    "v_add_f32 v37, v37, v57\n"
    "v_add_f32 v38, v38, v58\n"
    "v_add_f32 v39, v39, v59\n"
    "s_waitcnt lgkmcnt(3)\n"
    "v_add_f32 v40, v40, v60\n"
    "v_add_f32 v41, v41, v61\n"
    "v_add_f32 v42, v42, v62\n"
    "v_add_f32 v43, v43, v63\n"
    "s_waitcnt lgkmcnt(2)\n"
    "v_add_f32 v44, v44, v64\n"
    "v_add_f32 v45, v45, v65\n"
    "v_add_f32 v46, v46, v66\n"
    "v_add_f32 v47, v47, v67\n"
    "s_waitcnt lgkmcnt(1)\n"
    "v_add_f32 v48, v48, v68\n"
    "v_add_f32 v49, v49, v69\n"
    "v_add_f32 v50, v50, v70\n"
    "v_add_f32 v51, v51, v71\n"
    "s_waitcnt lgkmcnt(0)\n"
    "v_add_f32 v52, v52, v72\n"
    "v_add_f32 v53, v53, v73\n"
    "v_add_f32 v54, v54, v74\n"
    "v_add_f32 v55, v55, v75\n"
    "s_mov_b64 exec, 1\n"
    "v_mov_b32 v14, 161044\n"
    "v_mov_b32 v15, 1\n"
    "ds_add_u32 v14, v15\n"
    "s_mov_b64 exec, -1\n"
    "v_cvt_pk_bf16_f32 v56, v36, v37\n"
    "v_cvt_pk_bf16_f32 v57, v38, v39\n"
    "v_cvt_pk_bf16_f32 v58, v40, v41\n"
    "v_cvt_pk_bf16_f32 v60, v42, v43\n"
    "v_cvt_pk_bf16_f32 v61, v44, v45\n"
    "v_cvt_pk_bf16_f32 v62, v46, v47\n"
    "v_cvt_pk_bf16_f32 v64, v48, v49\n"
    "v_cvt_pk_bf16_f32 v65, v50, v51\n"
    "v_cvt_pk_bf16_f32 v66, v52, v53\n"
    "v_cvt_pk_bf16_f32 v68, v54, v55\n"
    "v_mov_b32 v59, s73\n"
    "v_mov_b32 v63, s73\n"
    "v_mov_b32 v67, s73\n"
    "v_mov_b32 v69, s73\n"
    "buffer_store_dwordx4 v[56:59], v3, s[24:27], s57 offen offset:0 sc1\n"
    "buffer_store_dwordx4 v[60:63], v3, s[24:27], s57 offen offset:1024 sc1\n"
    "buffer_store_dwordx4 v[64:67], v3, s[24:27], s57 offen offset:2048 sc1\n"
    "v_lshlrev_b32 v70, 3, v0\n"
    "buffer_store_dwordx2 v[68:69], v70, s[24:27], s57 offen offset:3072 sc1\n"
    "s_setprio 0\n"
    "s_waitcnt vmcnt(15)\n"
    "v_mfma_f32_16x16x32_bf16 v[36:39], v[152:155], v[132:135], 0\n"
    "v_mfma_f32_16x16x32_bf16 v[40:43], v[156:159], v[132:135], 0\n"
    "v_mfma_f32_16x16x32_bf16 v[44:47], v[160:163], v[132:135], 0\n"
    "v_mfma_f32_16x16x32_bf16 v[48:51], v[164:167], v[132:135], 0\n"
    "v_mfma_f32_16x16x32_bf16 v[52:55], v[168:171], v[132:135], 0\n"
    "buffer_load_dwordx4 v[128:131], v6, s[24:27], s48 offen sc1\n"
    "s_waitcnt vmcnt(15)\n"
    "v_mfma_f32_16x16x32_bf16 v[56:59], v[152:155], v[252:255], 0\n"
    "v_mfma_f32_16x16x32_bf16 v[60:63], v[156:159], v[252:255], 0\n"
    "v_mfma_f32_16x16x32_bf16 v[64:67], v[160:163], v[252:255], 0\n"
    "v_mfma_f32_16x16x32_bf16 v[68:71], v[164:167], v[252:255], 0\n"
    "v_mfma_f32_16x16x32_bf16 v[72:75], v[168:171], v[252:255], 0\n"
    "buffer_load_dwordx4 v[132:135], v6, s[24:27], s49 offen sc1\n"
    "s_waitcnt vmcnt(15)\n"
    "v_mfma_f32_16x16x32_bf16 v[76:79], v[152:155], v[136:139], 0\n"
    "v_mfma_f32_16x16x32_bf16 v[80:83], v[156:159], v[136:139], 0\n"
    "v_mfma_f32_16x16x32_bf16 v[84:87], v[160:163], v[136:139], 0\n"
    "v_mfma_f32_16x16x32_bf16 v[88:91], v[164:167], v[136:139], 0\n"
    "v_mfma_f32_16x16x32_bf16 v[92:95], v[168:171], v[136:139], 0\n"
    "buffer_load_dwordx4 v[252:255], v7, s[24:27], s48 offen sc1\n"
    "s_waitcnt vmcnt(15)\n"
    "v_mfma_f32_16x16x32_bf16 v[96:99], v[152:155], v[140:143], 0\n"
    "v_mfma_f32_16x16x32_bf16 v[100:103], v[156:159], v[140:143], 0\n"
    "v_mfma_f32_16x16x32_bf16 v[104:107], v[160:163], v[140:143], 0\n"
    "v_mfma_f32_16x16x32_bf16 v[108:111], v[164:167], v[140:143], 0\n"
    "v_mfma_f32_16x16x32_bf16 v[112:115], v[168:171], v[140:143], 0\n"
    "buffer_load_dwordx4 v[136:139], v7, s[24:27], s49 offen sc1\n"
    "s_waitcnt vmcnt(15)\n"
    "v_mfma_f32_16x16x32_bf16 v[36:39], v[172:175], v[144:147], v[36:39]\n"
    "v_mfma_f32_16x16x32_bf16 v[40:43], v[176:179], v[144:147], v[40:43]\n"
    "v_mfma_f32_16x16x32_bf16 v[44:47], v[180:183], v[144:147], v[44:47]\n"
    "v_mfma_f32_16x16x32_bf16 v[48:51], v[184:187], v[144:147], v[48:51]\n"
    "v_mfma_f32_16x16x32_bf16 v[52:55], v[188:191], v[144:147], v[52:55]\n"
    "buffer_load_dwordx4 v[152:155], v8, s[24:27], s48 offen sc1\n"
    "s_waitcnt vmcnt(15)\n"
    "v_mfma_f32_16x16x32_bf16 v[56:59], v[172:175], v[148:151], v[56:59]\n"
    "v_mfma_f32_16x16x32_bf16 v[60:63], v[176:179], v[148:151], v[60:63]\n"
    "v_mfma_f32_16x16x32_bf16 v[64:67], v[180:183], v[148:151], v[64:67]\n"
    "v_mfma_f32_16x16x32_bf16 v[68:71], v[184:187], v[148:151], v[68:71]\n"
    "v_mfma_f32_16x16x32_bf16 v[72:75], v[188:191], v[148:151], v[72:75]\n"
    "buffer_load_dwordx4 v[156:159], v8, s[24:27], s49 offen sc1\n"
    "s_waitcnt vmcnt(15)\n"
    "v_mfma_f32_16x16x32_bf16 v[76:79], v[172:175], v[16:19], v[76:79]\n"
    "v_mfma_f32_16x16x32_bf16 v[80:83], v[176:179], v[16:19], v[80:83]\n"
    "v_mfma_f32_16x16x32_bf16 v[84:87], v[180:183], v[16:19], v[84:87]\n"
    "v_mfma_f32_16x16x32_bf16 v[88:91], v[184:187], v[16:19], v[88:91]\n"
    "v_mfma_f32_16x16x32_bf16 v[92:95], v[188:191], v[16:19], v[92:95]\n"
    "buffer_load_dwordx4 v[160:163], v5, s[24:27], s48 offen sc1\n"
    "s_waitcnt vmcnt(15)\n"
    "v_mfma_f32_16x16x32_bf16 v[96:99], v[172:175], v[20:23], v[96:99]\n"
    "v_mfma_f32_16x16x32_bf16 v[100:103], v[176:179], v[20:23], v[100:103]\n"
    "v_mfma_f32_16x16x32_bf16 v[104:107], v[180:183], v[20:23], v[104:107]\n"
    "v_mfma_f32_16x16x32_bf16 v[108:111], v[184:187], v[20:23], v[108:111]\n"
    "v_mfma_f32_16x16x32_bf16 v[112:115], v[188:191], v[20:23], v[112:115]\n"
    "buffer_load_dwordx4 v[164:167], v5, s[24:27], s49 offen sc1\n"
    "v_lshlrev_b32 v150, 3, v0\n"
    "v_mov_b32 v14, 161044\n"
    "s_mov_b32 s0, 524288\n"
    "s_waitcnt lgkmcnt(0)\n"
    "Lk_lw_36:\n"
    "ds_read_b32 v15, v14\n"
    "s_waitcnt lgkmcnt(0)\n"
    "v_readfirstlane_b32 s92, v15\n"
    "s_nop 1\n"
    "s_cmp_ge_u32 s92, 4\n"
    "s_cbranch_scc1 Lk_lwd_37\n"
    "s_sub_u32 s0, s0, 1\n"
    "s_cbranch_scc0 Lk_lw_36\n"
    "Lk_lwd_37:\n"
    "s_waitcnt vmcnt(15)\n"
    "v_mfma_f32_16x16x32_bf16 v[36:39], v[192:195], v[24:27], v[36:39]\n"
    "v_mfma_f32_16x16x32_bf16 v[40:43], v[196:199], v[24:27], v[40:43]\n"
    "v_mfma_f32_16x16x32_bf16 v[44:47], v[200:203], v[24:27], v[44:47]\n"
    "v_mfma_f32_16x16x32_bf16 v[48:51], v[204:207], v[24:27], v[48:51]\n"
    "v_mfma_f32_16x16x32_bf16 v[52:55], v[208:211], v[24:27], v[52:55]\n"
    "s_waitcnt vmcnt(14)\n"
    "v_mfma_f32_16x16x32_bf16 v[56:59], v[192:195], v[28:31], v[56:59]\n"
    "v_mfma_f32_16x16x32_bf16 v[60:63], v[196:199], v[28:31], v[60:63]\n"
    "v_mfma_f32_16x16x32_bf16 v[64:67], v[200:203], v[28:31], v[64:67]\n"
    "v_mfma_f32_16x16x32_bf16 v[68:71], v[204:207], v[28:31], v[68:71]\n"
    "v_mfma_f32_16x16x32_bf16 v[72:75], v[208:211], v[28:31], v[72:75]\n"
    "s_waitcnt vmcnt(13)\n"
    "v_mfma_f32_16x16x32_bf16 v[76:79], v[192:195], v[32:35], v[76:79]\n"
    "v_mfma_f32_16x16x32_bf16 v[80:83], v[196:199], v[32:35], v[80:83]\n"
    "v_mfma_f32_16x16x32_bf16 v[84:87], v[200:203], v[32:35], v[84:87]\n"
    "v_mfma_f32_16x16x32_bf16 v[88:91], v[204:207], v[32:35], v[88:91]\n"
    "v_mfma_f32_16x16x32_bf16 v[92:95], v[208:211], v[32:35], v[92:95]\n"
    "s_waitcnt vmcnt(12)\n"
    "v_mfma_f32_16x16x32_bf16 v[96:99], v[192:195], v[124:127], v[96:99]\n"
    "v_mfma_f32_16x16x32_bf16 v[100:103], v[196:199], v[124:127], v[100:103]\n"
    "v_mfma_f32_16x16x32_bf16 v[104:107], v[200:203], v[124:127], v[104:107]\n"
    "v_mfma_f32_16x16x32_bf16 v[108:111], v[204:207], v[124:127], v[108:111]\n"
    "v_mfma_f32_16x16x32_bf16 v[112:115], v[208:211], v[124:127], v[112:115]\n"
    "s_waitcnt vmcnt(7)\n"
    "v_mfma_f32_16x16x32_bf16 v[56:59], v[212:215], v[128:131], v[56:59]\n"
    "v_mfma_f32_16x16x32_bf16 v[60:63], v[216:219], v[128:131], v[60:63]\n"
    "v_mfma_f32_16x16x32_bf16 v[64:67], v[220:223], v[128:131], v[64:67]\n"
    "v_mfma_f32_16x16x32_bf16 v[68:71], v[224:227], v[128:131], v[68:71]\n"
    "v_mfma_f32_16x16x32_bf16 v[72:75], v[228:231], v[128:131], v[72:75]\n"
    "s_waitcnt vmcnt(6)\n"
    "v_mfma_f32_16x16x32_bf16 v[56:59], v[232:235], v[132:135], v[56:59]\n"
    "v_mfma_f32_16x16x32_bf16 v[60:63], v[236:239], v[132:135], v[60:63]\n"
    "v_mfma_f32_16x16x32_bf16 v[64:67], v[240:243], v[132:135], v[64:67]\n"
    "v_mfma_f32_16x16x32_bf16 v[68:71], v[244:247], v[132:135], v[68:71]\n"
    "v_mfma_f32_16x16x32_bf16 v[72:75], v[248:251], v[132:135], v[72:75]\n"
    "s_waitcnt vmcnt(5)\n"
    "v_mfma_f32_16x16x32_bf16 v[76:79], v[212:215], v[252:255], v[76:79]\n"
    "v_mfma_f32_16x16x32_bf16 v[80:83], v[216:219], v[252:255], v[80:83]\n"
    "s_nop 0\n"
    "ds_write_b128 v121, v[56:59] offset:0\n"
    "v_mfma_f32_16x16x32_bf16 v[84:87], v[220:223], v[252:255], v[84:87]\n"
    "ds_write_b128 v121, v[60:63] offset:1024\n"
    "v_mfma_f32_16x16x32_bf16 v[88:91], v[224:227], v[252:255], v[88:91]\n"
    "ds_write_b128 v121, v[64:67] offset:2048\n"
    "v_mfma_f32_16x16x32_bf16 v[92:95], v[228:231], v[252:255], v[92:95]\n"
    "ds_write_b128 v121, v[68:71] offset:3072\n"
    "s_waitcnt vmcnt(4)\n"
    "v_mfma_f32_16x16x32_bf16 v[76:79], v[232:235], v[136:139], v[76:79]\n"
    "v_mfma_f32_16x16x32_bf16 v[80:83], v[236:239], v[136:139], v[80:83]\n"
    "v_mfma_f32_16x16x32_bf16 v[84:87], v[240:243], v[136:139], v[84:87]\n"
    "v_mfma_f32_16x16x32_bf16 v[88:91], v[244:247], v[136:139], v[88:91]\n"
    "v_mfma_f32_16x16x32_bf16 v[92:95], v[248:251], v[136:139], v[92:95]\n"
    "ds_write_b128 v121, v[72:75] offset:4096\n"
    "s_waitcnt vmcnt(3)\n"
    "v_mfma_f32_16x16x32_bf16 v[96:99], v[212:215], v[152:155], v[96:99]\n"
    "v_mfma_f32_16x16x32_bf16 v[100:103], v[216:219], v[152:155], v[100:103]\n"
    "ds_write_b128 v122, v[76:79] offset:0\n"
    "v_mfma_f32_16x16x32_bf16 v[104:107], v[220:223], v[152:155], v[104:107]\n"
    "ds_write_b128 v122, v[80:83] offset:1024\n"
    "v_mfma_f32_16x16x32_bf16 v[108:111], v[224:227], v[152:155], v[108:111]\n"
    "ds_write_b128 v122, v[84:87] offset:2048\n"
    "v_mfma_f32_16x16x32_bf16 v[112:115], v[228:231], v[152:155], v[112:115]\n"
    "ds_write_b128 v122, v[88:91] offset:3072\n"
    "s_waitcnt vmcnt(2)\n"
    "v_mfma_f32_16x16x32_bf16 v[96:99], v[232:235], v[156:159], v[96:99]\n"
    "v_mfma_f32_16x16x32_bf16 v[100:103], v[236:239], v[156:159], v[100:103]\n"
    "v_mfma_f32_16x16x32_bf16 v[104:107], v[240:243], v[156:159], v[104:107]\n"
    "v_mfma_f32_16x16x32_bf16 v[108:111], v[244:247], v[156:159], v[108:111]\n"
    "v_mfma_f32_16x16x32_bf16 v[112:115], v[248:251], v[156:159], v[112:115]\n"
    "ds_write_b128 v122, v[92:95] offset:4096\n"
    "s_waitcnt vmcnt(1)\n"
    "v_mfma_f32_16x16x32_bf16 v[36:39], v[212:215], v[160:163], v[36:39]\n"
    "s_nop 0\n"
    "ds_write_b128 v123, v[96:99] offset:0\n"
    "v_mfma_f32_16x16x32_bf16 v[40:43], v[216:219], v[160:163], v[40:43]\n"
    "ds_write_b128 v123, v[100:103] offset:1024\n"
    "v_mfma_f32_16x16x32_bf16 v[44:47], v[220:223], v[160:163], v[44:47]\n"
    "ds_write_b128 v123, v[104:107] offset:2048\n"
    "v_mfma_f32_16x16x32_bf16 v[48:51], v[224:227], v[160:163], v[48:51]\n"
    "ds_write_b128 v123, v[108:111] offset:3072\n"
    "v_mfma_f32_16x16x32_bf16 v[52:55], v[228:231], v[160:163], v[52:55]\n"
    "ds_write_b128 v123, v[112:115] offset:4096\n"
    "s_waitcnt vmcnt(0)\n"
    "v_mfma_f32_16x16x32_bf16 v[36:39], v[232:235], v[164:167], v[36:39]\n"
    "v_mfma_f32_16x16x32_bf16 v[40:43], v[236:239], v[164:167], v[40:43]\n"
    "v_mfma_f32_16x16x32_bf16 v[44:47], v[240:243], v[164:167], v[44:47]\n"
    "v_mfma_f32_16x16x32_bf16 v[48:51], v[244:247], v[164:167], v[48:51]\n"
    "v_mfma_f32_16x16x32_bf16 v[52:55], v[248:251], v[164:167], v[52:55]\n"
    "buffer_load_dwordx4 v[168:171], v3, s[24:27], s58 offen offset:0 sc1\n"
    "buffer_load_dwordx4 v[140:143], v3, s[24:27], s58 offen offset:1024 sc1\n"
    "buffer_load_dwordx4 v[144:147], v3, s[24:27], s58 offen offset:2048 sc1\n"
    "buffer_load_dwordx2 v[148:149], v150, s[24:27], s58 offen offset:3072 sc1\n"
    "s_waitcnt lgkmcnt(0)\n"
    "s_barrier\n"
    "v_mul_u32_u24 v5, 336, v1\n"
    "v_lshl_add_u32 v5, v2, 4, v5\n"
    "v_add_u32 v5, s33, v5\n"
    "s_mul_i32 s92, s68, 15360\n"
    "s_add_u32 s92, s92, 81920\n"
    "v_add_u32 v14, s92, v3\n"
    "ds_read_b128 v[56:59], v14 offset:0\n"
    "ds_read_b128 v[60:63], v14 offset:1024\n"
    "ds_read_b128 v[64:67], v14 offset:2048\n"
    "ds_read_b128 v[68:71], v14 offset:3072\n"
    "ds_read_b128 v[72:75], v14 offset:4096\n"
    "ds_read_b128 v[76:79], v14 offset:5120\n"
    "ds_read_b128 v[80:83], v14 offset:6144\n"
    "ds_read_b128 v[84:87], v14 offset:7168\n"
    "ds_read_b128 v[88:91], v14 offset:8192\n"
    "ds_read_b128 v[92:95], v14 offset:9216\n"
    "ds_read_b128 v[96:99], v14 offset:10240\n"
    "ds_read_b128 v[100:103], v14 offset:11264\n"
    "ds_read_b128 v[104:107], v14 offset:12288\n"
    "ds_read_b128 v[108:111], v14 offset:13312\n"
    "ds_read_b128 v[112:115], v14 offset:14336\n"
    "s_waitcnt lgkmcnt(14)\n"
    "v_pk_add_f32 v[36:37], v[36:37], v[56:57]\n"
    "v_pk_add_f32 v[38:39], v[38:39], v[58:59]\n"
    "s_waitcnt lgkmcnt(13)\n"
    "v_pk_add_f32 v[40:41], v[40:41], v[60:61]\n"
    "v_pk_add_f32 v[42:43], v[42:43], v[62:63]\n"
    "s_waitcnt lgkmcnt(12)\n"
    "v_pk_add_f32 v[44:45], v[44:45], v[64:65]\n"
    "v_pk_add_f32 v[46:47], v[46:47], v[66:67]\n"
    "s_waitcnt lgkmcnt(11)\n"
    "v_pk_add_f32 v[48:49], v[48:49], v[68:69]\n"
    "v_pk_add_f32 v[50:51], v[50:51], v[70:71]\n"
    "s_waitcnt lgkmcnt(10)\n"
    "v_pk_add_f32 v[52:53], v[52:53], v[72:73]\n"
    "v_pk_add_f32 v[54:55], v[54:55], v[74:75]\n"
    "s_mul_i32 s92, s68, 5120\n"
    "s_add_u32 s92, s92, 61440\n"
    "v_add_u32 v14, s92, v3\n"
    "ds_read_b128 v[56:59], v14 offset:0\n"
    "ds_read_b128 v[60:63], v14 offset:1024\n"
    "ds_read_b128 v[64:67], v14 offset:2048\n"
    "ds_read_b128 v[68:71], v14 offset:3072\n"
    "ds_read_b128 v[72:75], v14 offset:4096\n"
    "s_waitcnt lgkmcnt(14)\n"
    "v_pk_add_f32 v[36:37], v[36:37], v[76:77]\n"
    "v_pk_add_f32 v[38:39], v[38:39], v[78:79]\n"
    "s_waitcnt lgkmcnt(13)\n"
    "v_pk_add_f32 v[40:41], v[40:41], v[80:81]\n"
    "v_pk_add_f32 v[42:43], v[42:43], v[82:83]\n"
    "s_waitcnt lgkmcnt(12)\n"
    "v_pk_add_f32 v[44:45], v[44:45], v[84:85]\n"
    "v_pk_add_f32 v[46:47], v[46:47], v[86:87]\n"
    "s_waitcnt lgkmcnt(11)\n"
    "v_pk_add_f32 v[48:49], v[48:49], v[88:89]\n"
    "v_pk_add_f32 v[50:51], v[50:51], v[90:91]\n"
    "s_waitcnt lgkmcnt(10)\n"
    "v_pk_add_f32 v[52:53], v[52:53], v[92:93]\n"
    "v_pk_add_f32 v[54:55], v[54:55], v[94:95]\n"
    "s_waitcnt lgkmcnt(9)\n"
    "v_pk_add_f32 v[36:37], v[36:37], v[96:97]\n"
    "v_pk_add_f32 v[38:39], v[38:39], v[98:99]\n"
    "s_waitcnt lgkmcnt(8)\n"
    "v_pk_add_f32 v[40:41], v[40:41], v[100:101]\n"
    "v_pk_add_f32 v[42:43], v[42:43], v[102:103]\n"
    "s_waitcnt lgkmcnt(7)\n"
    "v_pk_add_f32 v[44:45], v[44:45], v[104:105]\n"
    "v_pk_add_f32 v[46:47], v[46:47], v[106:107]\n"
    "s_waitcnt lgkmcnt(6)\n"
    "v_pk_add_f32 v[48:49], v[48:49], v[108:109]\n"
    "v_pk_add_f32 v[50:51], v[50:51], v[110:111]\n"
    "s_waitcnt lgkmcnt(5)\n"
    "v_pk_add_f32 v[52:53], v[52:53], v[112:113]\n"
    "v_pk_add_f32 v[54:55], v[54:55], v[114:115]\n"
    "s_waitcnt lgkmcnt(4)\n"
    "v_pk_add_f32 v[36:37], v[36:37], v[56:57]\n"
    "v_pk_add_f32 v[38:39], v[38:39], v[58:59]\n"
    "s_waitcnt lgkmcnt(3)\n"
    "v_pk_add_f32 v[40:41], v[40:41], v[60:61]\n"
    "v_pk_add_f32 v[42:43], v[42:43], v[62:63]\n"
    "s_waitcnt lgkmcnt(2)\n"
    "v_pk_add_f32 v[44:45], v[44:45], v[64:65]\n"
    "v_pk_add_f32 v[46:47], v[46:47], v[66:67]\n"
    "s_waitcnt lgkmcnt(1)\n"
    "v_pk_add_f32 v[48:49], v[48:49], v[68:69]\n"
    "v_pk_add_f32 v[50:51], v[50:51], v[70:71]\n"
    "s_waitcnt lgkmcnt(0)\n"
    "v_pk_add_f32 v[52:53], v[52:53], v[72:73]\n"
    "v_pk_add_f32 v[54:55], v[54:55], v[74:75]\n"
    "s_waitcnt vmcnt(3)\n"
    "v_xor_b32 v56, s73, v171\n"
    "s_waitcnt vmcnt(2)\n"
    "v_xor_b32 v57, s73, v143\n"
    "s_waitcnt vmcnt(1)\n"
    "v_xor_b32 v58, s73, v147\n"
    "s_waitcnt vmcnt(0)\n"
    "v_xor_b32 v59, s73, v149\n"
    "v_or_b32 v56, v56, v57\n"
    "v_or3_b32 v56, v56, v58, v59\n"
    "v_cmp_ne_u32 vcc, 0, v56\n"
    "s_nop 4\n"
    "s_cbranch_vccz Lk_skip_38\n"
    "s_mov_b32 s0, 131072\n"
    "s_nop 4\n"
    "Lk_poll_39:\n"
    "buffer_load_dwordx4 v[168:171], v3, s[24:27], s58 offen offset:0 sc1\n"
    "buffer_load_dwordx4 v[140:143], v3, s[24:27], s58 offen offset:1024 sc1\n"
    "buffer_load_dwordx4 v[144:147], v3, s[24:27], s58 offen offset:2048 sc1\n"
    "buffer_load_dwordx2 v[148:149], v150, s[24:27], s58 offen offset:3072 sc1\n"
    "Lk_chk_41:\n"
    "s_waitcnt vmcnt(0)\n"
    "v_xor_b32 v56, s73, v171\n"
    "v_xor_b32 v57, s73, v143\n"
    "v_xor_b32 v58, s73, v147\n"
    "v_xor_b32 v59, s73, v149\n"
    "v_or_b32 v56, v56, v57\n"
    "v_or3_b32 v56, v56, v58, v59\n"
    "v_cmp_ne_u32 vcc, 0, v56\n"
    "s_nop 4\n"
    "s_cbranch_vccz Lk_done_40\n"
    "s_sleep 4\n"
    "s_sub_u32 s0, s0, 1\n"
    "s_cbranch_scc0 Lk_poll_39\n"
    "Lk_done_40:\n"
    "Lk_skip_38:\n"
    "v_lshlrev_b32 v56, 16, v168\n"
    "v_and_b32 v57, s1, v168\n"
    "v_lshlrev_b32 v58, 16, v169\n"
    "v_and_b32 v59, s1, v169\n"
    "v_pk_add_f32 v[36:37], v[36:37], v[56:57]\n"
    "v_pk_add_f32 v[38:39], v[38:39], v[58:59]\n"
    "ds_write_b128 v5, v[36:39] offset:0\n"
    "v_lshlrev_b32 v60, 16, v170\n"
    "v_and_b32 v61, s1, v170\n"
    "v_lshlrev_b32 v62, 16, v140\n"
    "v_and_b32 v63, s1, v140\n"
    "v_pk_add_f32 v[40:41], v[40:41], v[60:61]\n"
    "v_pk_add_f32 v[42:43], v[42:43], v[62:63]\n"
    "ds_write_b128 v5, v[40:43] offset:64\n"
    "v_lshlrev_b32 v64, 16, v141\n"
    "v_and_b32 v65, s1, v141\n"
    "v_lshlrev_b32 v66, 16, v142\n"
    "v_and_b32 v67, s1, v142\n"
    "v_pk_add_f32 v[44:45], v[44:45], v[64:65]\n"
    "v_pk_add_f32 v[46:47], v[46:47], v[66:67]\n"
    "ds_write_b128 v5, v[44:47] offset:128\n"
    "v_lshlrev_b32 v68, 16, v144\n"
    "v_and_b32 v69, s1, v144\n"
    "v_lshlrev_b32 v70, 16, v145\n"
    "v_and_b32 v71, s1, v145\n"
    "v_pk_add_f32 v[48:49], v[48:49], v[68:69]\n"
    "v_pk_add_f32 v[50:51], v[50:51], v[70:71]\n"
    "ds_write_b128 v5, v[48:51] offset:192\n"
    "v_lshlrev_b32 v72, 16, v146\n"
    "v_and_b32 v73, s1, v146\n"
    "v_lshlrev_b32 v74, 16, v148\n"
    "v_and_b32 v75, s1, v148\n"
    "v_pk_add_f32 v[52:53], v[52:53], v[72:73]\n"
    "v_pk_add_f32 v[54:55], v[54:55], v[74:75]\n"
    "ds_write_b128 v5, v[52:55] offset:256\n"
    "ds_read_b128 v[76:79], v9\n"
    "ds_read_b128 v[80:83], v10\n"
    "ds_read_b128 v[84:87], v11\n"
    "ds_read_b128 v[88:91], v12\n"
    "ds_read_b128 v[92:95], v13\n"
    "s_waitcnt lgkmcnt(4)\n"
    "buffer_store_dwordx4 v[76:79], v116, s[64:67], s31 offen sc0 sc1\n"
    "s_waitcnt lgkmcnt(3)\n"
    "buffer_store_dwordx4 v[80:83], v117, s[64:67], s31 offen sc0 sc1\n"
    "s_waitcnt lgkmcnt(2)\n"
    "buffer_store_dwordx4 v[84:87], v118, s[64:67], s31 offen sc0 sc1\n"
    "s_waitcnt lgkmcnt(1)\n"
    "buffer_store_dwordx4 v[88:91], v119, s[64:67], s31 offen sc0 sc1\n"
    "s_waitcnt lgkmcnt(0)\n"
    "buffer_store_dwordx4 v[92:95], v120, s[64:67], s31 offen sc0 sc1\n"
    "s_endpgm\n"
    "Lk_roleM_1:\n"
    "s_setprio 2\n"
    "s_mov_b32 s82, Lk_end-Lk_begin\n"
    "s_mov_b32 s83, 0x20000\n"
    "s_lshr_b32 s92, s6, 3\n"
    "s_lshl_b32 s92, s92, 2\n"
    "s_add_u32 s92, s92, s68\n"
    "s_lshl_b32 s92, s92, 9\n"
    "v_and_b32 v197, 3, v0\n"
    "v_lshlrev_b32 v197, 7, v197\n"
    "buffer_load_dword v197, v197, s[80:83], s92 offen\n"
    "v_min_u32 v28, 39, v0\n"
    "v_lshlrev_b32 v28, 1, v28\n"
    "s_mul_i32 s92, s70, 80\n"
    "v_min_u32 v30, 11, v1\n"
    "v_lshlrev_b32 v30, 2, v30\n"
    "s_mul_i32 s93, s70, 192\n"
    "s_mul_i32 s94, s68, 48\n"
    "s_add_u32 s93, s93, s94\n"
    "v_lshrrev_b32 v31, 5, v0\n"
    "v_mul_u32_u24 v31, 1280, v31\n"
    "v_and_b32 v29, 31, v0\n"
    "v_lshl_add_u32 v202, v29, 3, v31\n"
    "s_lshl_b32 s3, s69, 4\n"
    "s_add_u32 s3, s3, s70\n"
    "s_lshl_b32 s3, s3, 2\n"
    "s_add_u32 s3, s3, s68\n"
    "s_mul_i32 s2, s3, 2560\n"
    "s_and_b32 s94, s68, 1\n"
    "s_lshl_b32 s94, s94, 8\n"
    "s_add_u32 s94, s94, s6\n"
    "s_mul_i32 s75, s94, 6554\n"
    "s_lshr_b32 s75, s75, 16\n"
    "s_mul_i32 s95, s75, 10\n"
    "s_sub_u32 s76, s94, s95\n"
    "s_cmp_lt_u32 s68, 2\n"
    "s_cselect_b32 s78, 80, 0\n"
    "s_cselect_b32 s4, 40960, 0\n"
    "s_add_u32 s78, s78, 400\n"
    "s_cmp_lt_u32 s94, s78\n"
    "s_cselect_b32 s78, 1, 0\n"
    "s_max_u32 s4, s4, 20480\n"
    "s_mul_i32 s4, s4, s75\n"
    "s_lshl_b32 s95, s76, 7\n"
    "s_add_u32 s4, s4, s95\n"
    "s_cmp_eq_u32 s78, 0\n"
    "s_cselect_b32 s4, 0x40000000, s4\n"
    "v_lshrrev_b32 v248, 3, v0\n"
    "v_and_b32 v249, 7, v0\n"
    "v_mul_u32_u24 v248, 1280, v248\n"
    "v_lshl_add_u32 v248, v249, 4, v248\n"
    "s_lshl_b32 s5, s68, 12\n"
    "s_waitcnt lgkmcnt(0)\n"
    "s_load_dword s73, s[24:25], 0\n"
    "buffer_load_dwordx2 v[172:173], v202, s[8:11], s2 offen offset:0\n"
    "buffer_load_dwordx2 v[176:177], v202, s[8:11], s2 offen offset:256\n"
    "buffer_load_dwordx2 v[180:181], v202, s[8:11], s2 offen offset:512\n"
    "buffer_load_dwordx2 v[184:185], v202, s[8:11], s2 offen offset:768\n"
    "buffer_load_dwordx2 v[188:189], v202, s[8:11], s2 offen offset:1024\n"
    "buffer_load_ushort v24, v28, s[40:43], s92 offen\n"
    "buffer_load_ushort v25, v28, s[44:47], s92 offen\n"
    "buffer_load_ushort v26, v28, s[48:51], s92 offen\n"
    "buffer_load_dword v13, v30, s[52:55], s93 offen\n"
    "s_cmp_lt_u32 s68, 2\n"
    "s_cselect_b64 s[80:81], s[28:29], s[32:33]\n"
    "s_cselect_b64 s[82:83], s[30:31], s[34:35]\n"
    "s_cselect_b64 s[84:85], s[28:29], s[36:37]\n"
    "s_cselect_b64 s[86:87], s[30:31], s[38:39]\n"
    "s_cselect_b32 s94, 20480, 0\n"
    "s_add_u32 s23, s4, 10240\n"
    "s_add_u32 s39, s4, s94\n"
    "s_add_u32 s55, s39, 10240\n"
    "s_setprio 0\n"
    "v_mov_b32 v236, 0\n"
    "v_mov_b32 v237, 0\n"
    "v_mov_b32 v238, 0\n"
    "v_mov_b32 v239, 0\n"
    "v_mov_b32 v31, 161024\n"
    "ds_write_b128 v31, v[236:239]\n"
    "ds_write_b128 v31, v[236:239] offset:16\n"
    "v_mov_b32 v31, 137984\n"
    "ds_write_b128 v31, v[236:239]\n"
    "ds_write_b128 v31, v[236:239] offset:256\n"
    "v_mov_b32 v32, 0x3f803f80\n"
    "v_mov_b32 v33, 0x3f803f80\n"
    "v_mov_b32 v34, 0x3f803f80\n"
    "v_mov_b32 v35, 0x3f803f80\n"
    "v_lshrrev_b32 v30, 2, v1\n"
    "v_lshlrev_b32 v31, 1, v30\n"
    "v_bfe_u32 v29, v1, 1, 1\n"
    "v_or_b32 v31, v31, v29\n"
    "v_xor_b32 v31, v31, v2\n"
    "v_lshlrev_b32 v31, 4, v31\n"
    "v_lshlrev_b32 v30, 3, v30\n"
    "v_and_b32 v29, 3, v1\n"
    "v_add_u32 v30, v30, v29\n"
    "v_lshl_add_u32 v5, v30, 7, v31\n"
    "s_lshl_b32 s92, s68, 12\n"
    "v_add_u32 v5, s92, v5\n"
    "v_xor_b32 v6, 64, v5\n"
    "v_add_u32 v7, 77824, v5\n"
    "v_add_u32 v8, 77824, v6\n"
    "v_and_b32 v29, 1, v1\n"
    "v_lshrrev_b32 v30, 1, v1\n"
    "v_and_b32 v31, 6, v30\n"
    "v_or_b32 v31, v31, v29\n"
    "v_xor_b32 v31, v31, v2\n"
    "v_lshlrev_b32 v31, 4, v31\n"
    "v_mad_u32_u24 v29, v29, 40, v30\n"
    "v_lshl_add_u32 v29, v29, 7, v31\n"
    "v_add_u32 v244, 16384, v29\n"
    "v_xor_b32 v245, 64, v244\n"
    "v_add_u32 v246, 77824, v244\n"
    "v_add_u32 v247, 77824, v245\n"
    "v_lshrrev_b32 v29, 2, v1\n"
    "v_lshl_add_u32 v29, v2, 3, v29\n"
    "v_mul_u32_u24 v29, 192, v29\n"
    "v_and_b32 v30, 3, v1\n"
    "v_lshl_add_u32 v29, v30, 3, v29\n"
    "v_add_u32 v29, 26624, v29\n"
    "v_and_b32 v30, 1, v2\n"
    "v_lshlrev_b32 v30, 5, v30\n"
    "v_add_u32 v9, v29, v30\n"
    "v_sub_u32 v10, v29, v30\n"
    "v_add_u32 v11, 77824, v9\n"
    "v_add_u32 v12, 77824, v10\n"
    "s_mul_i32 s17, s70, 14080\n"
    "s_add_u32 s17, s17, 3866624\n"
    "s_and_b32 s22, s69, 3\n"
    "s_lshl_b32 s92, s22, 7\n"
    "s_add_u32 s92, s92, s74\n"
    "s_sub_u32 s93, s92, 1280\n"
    "s_cselect_b32 s92, s92, s93\n"
    "s_mul_i32 s92, s92, 11\n"
    "s_add_u32 s16, s17, s92\n"
    "s_lshl_b32 s22, s22, 2\n"
    "s_add_u32 s22, s22, s68\n"
    "s_and_b32 s93, s22, 2\n"
    "s_cmp_lt_u32 s22, 12\n"
    "s_cselect_b32 s93, s93, 0\n"
    "s_cmp_eq_u32 s93, 2\n"
    "s_cselect_b32 s16, s16, 0x40000000\n"
    "s_cmp_eq_u32 s68, 2\n"
    "s_cselect_b32 s92, 640, 0\n"
    "s_add_u32 s16, s16, s92\n"
    "s_cmp_eq_u32 s68, 2\n"
    "s_cselect_b32 s88, s16, 0x40000000\n"
    "v_lshlrev_b32 v249, 3, v1\n"
    "v_cmp_gt_u32 vcc, 16, v0\n"
    "v_mov_b32 v250, 0x40000000\n"
    "s_nop 0\n"
    "v_cndmask_b32 v249, v250, v249, vcc\n"
    "v_cmp_eq_u32 vcc, 0, v0\n"
    "s_nop 1\n"
    "v_cndmask_b32 v250, v250, 0, vcc\n"
    "s_cmp_lt_u32 s68, 2\n"
    "s_cbranch_scc0 Lk_skip_42\n"
    "s_lshl_b32 s92, s76, 2\n"
    "s_add_u32 s92, s92, 0\n"
    "s_mul_i32 s93, s92, 13108\n"
    "s_lshr_b32 s93, s93, 16\n"
    "s_mul_i32 s93, s93, 435\n"
    "s_add_u32 s92, s92, s93\n"
    "s_mul_i32 s93, s75, 5\n"
    "s_add_u32 s92, s92, s93\n"
    "s_lshl_b32 s92, s92, 10\n"
    "s_add_u32 s12, s92, 262144\n"
    "s_lshl_b32 s92, s76, 2\n"
    "s_add_u32 s92, s92, 1\n"
    "s_mul_i32 s93, s92, 13108\n"
    "s_lshr_b32 s93, s93, 16\n"
    "s_mul_i32 s93, s93, 435\n"
    "s_add_u32 s92, s92, s93\n"
    "s_mul_i32 s93, s75, 5\n"
    "s_add_u32 s92, s92, s93\n"
    "s_lshl_b32 s92, s92, 10\n"
    "s_add_u32 s13, s92, 262144\n"
    "s_lshl_b32 s92, s76, 2\n"
    "s_add_u32 s92, s92, 2\n"
    "s_mul_i32 s93, s92, 13108\n"
    "s_lshr_b32 s93, s93, 16\n"
    "s_mul_i32 s93, s93, 435\n"
    "s_add_u32 s92, s92, s93\n"
    "s_mul_i32 s93, s75, 5\n"
    "s_add_u32 s92, s92, s93\n"
    "s_lshl_b32 s92, s92, 10\n"
    "s_add_u32 s14, s92, 262144\n"
    "s_lshl_b32 s92, s76, 2\n"
    "s_add_u32 s92, s92, 3\n"
    "s_mul_i32 s93, s92, 13108\n"
    "s_lshr_b32 s93, s93, 16\n"
    "s_mul_i32 s93, s93, 435\n"
    "s_add_u32 s92, s92, s93\n"
    "s_mul_i32 s93, s75, 5\n"
    "s_add_u32 s92, s92, s93\n"
    "s_lshl_b32 s92, s92, 10\n"
    "s_add_u32 s15, s92, 262144\n"
    "s_mul_i32 s92, s75, 5462\n"
    "s_lshr_b32 s92, s92, 16\n"
    "s_mul_i32 s92, s92, 108\n"
    "s_add_u32 s92, s92, s75\n"
    "s_mul_i32 s93, s76, 12\n"
    "s_add_u32 s92, s92, s93\n"
    "s_lshl_b32 s92, s92, 2\n"
    "s_add_u32 s77, s92, 1024\n"
    "v_lshrrev_b32 v200, 2, v1\n"
    "v_lshl_add_u32 v200, v2, 2, v200\n"
    "v_and_b32 v201, 3, v1\n"
    "v_lshlrev_b32 v201, 3, v201\n"
    "v_lshl_add_u32 v200, v200, 7, v201\n"
    "v_add_u32 v200, s5, v200\n"
    "v_add_u32 v200, 116736, v200\n"
    "v_mov_b32 v201, v3\n"
    "Lk_skip_42:\n"
    "s_cmp_ge_u32 s68, 2\n"
    "s_cbranch_scc0 Lk_skip_43\n"
    "s_mul_i32 s93, s75, 13108\n"
    "s_lshr_b32 s93, s93, 16\n"
    "s_mul_i32 s93, s93, 435\n"
    "s_add_u32 s92, s75, s93\n"
    "s_mul_i32 s93, s76, 20\n"
    "s_add_u32 s92, s92, s93\n"
    "s_lshl_b32 s92, s92, 10\n"
    "s_add_u32 s12, s92, 507904\n"
    "s_add_u32 s13, s12, 10240\n"
    "s_add_u32 s14, s12, 256\n"
    "s_add_u32 s15, s13, 256\n"
    "s_mul_i32 s92, s75, 10\n"
    "s_add_u32 s92, s92, s76\n"
    "s_lshl_b32 s92, s92, 2\n"
    "s_add_u32 s77, s92, 3072\n"
    "v_lshrrev_b32 v201, 1, v2\n"
    "v_mul_u32_u24 v201, 5120, v201\n"
    "v_and_b32 v200, 1, v2\n"
    "v_lshl_add_u32 v201, v200, 9, v201\n"
    "v_lshl_add_u32 v201, v1, 4, v201\n"
    "v_lshlrev_b32 v200, 7, v1\n"
    "v_lshl_add_u32 v200, v2, 4, v200\n"
    "v_add_u32 v200, s5, v200\n"
    "v_add_u32 v200, 116736, v200\n"
    "Lk_skip_43:\n"
    "s_cmp_eq_u32 s78, 0\n"
    "s_cselect_b32 s12, 0x40000000, s12\n"
    "s_cselect_b32 s13, 0x40000000, s13\n"
    "s_cselect_b32 s14, 0x40000000, s14\n"
    "s_cselect_b32 s15, 0x40000000, s15\n"
    "s_cselect_b32 s77, 0x40000000, s77\n"
    "v_lshrrev_b32 v192, 1, v0\n"
    "s_lshl_b32 s3, s3, 5\n"
    "s_add_u32 s3, s3, 204800\n"
    "s_waitcnt lgkmcnt(0)\n"
    "s_mul_i32 s73, s73, 0x9E3779B1\n"
    "s_add_u32 s73, s73, 0x7F4A7C15\n"
    "s_waitcnt vmcnt(1)\n"
    "v_lshlrev_b32 v26, 16, v26\n"
    "v_mul_f32 v26, 0x3fb8aa3b, v26\n"
    "v_exp_f32 v26, v26\n"
    "v_lshlrev_b32 v24, 16, v24\n"
    "v_lshlrev_b32 v25, 16, v25\n"
    "v_cvt_pk_bf16_f32 v26, v26, v26\n"
    "s_mul_i32 s92, s68, 640\n"
    "v_and_b32 v26, s1, v26\n"
    "s_add_u32 s92, s92, 139008\n"
    "v_mul_f32 v27, v26, v24\n"
    "v_mul_f32 v28, v26, v25\n"
    "v_mul_f32 v27, 0x3fb8aa3b, v27\n"
    "v_mul_f32 v28, 0x3e22f983, v28\n"
    "v_add_u32 v193, s92, v3\n"
    "s_lshl_b32 s93, s68, 3\n"
    "v_cvt_f32_i32 v240, s93\n"
    "v_mul_f32 v241, v240, v27\n"
    "v_mul_f32 v242, v240, v28\n"
    "v_exp_f32 v241, v241\n"
    "v_fract_f32 v242, v242\n"
    "v_cos_f32 v204, v242\n"
    "v_sin_f32 v205, v242\n"
    "v_mul_f32 v204, v204, v241\n"
    "v_mul_f32 v205, v205, v241\n"
    "v_mov_b32 v206, v204\n"
    "v_xor_b32 v207, 0x80000000, v205\n"
    "s_lshl_b32 s93, s68, 5\n"
    "v_cvt_f32_i32 v240, s93\n"
    "v_mul_f32 v241, v240, v27\n"
    "v_mul_f32 v242, v240, v28\n"
    "v_exp_f32 v241, v241\n"
    "v_fract_f32 v242, v242\n"
    "v_cos_f32 v208, v242\n"
    "v_sin_f32 v209, v242\n"
    "v_mul_f32 v208, v208, v241\n"
    "v_mul_f32 v209, v209, v241\n"
    "v_mov_b32 v210, v208\n"
    "v_xor_b32 v211, 0x80000000, v209\n"
    "s_sub_u32 s93, s69, s68\n"
    "s_sub_u32 s93, s93, 1\n"
    "s_lshl_b32 s93, s93, 7\n"
    "s_cmp_lt_i32 s93, 0\n"
    "s_cselect_b32 s94, 0, 1.0\n"
    "s_max_i32 s93, s93, 0\n"
    "v_cvt_f32_i32 v240, s93\n"
    "v_mul_f32 v241, v240, v27\n"
    "v_mul_f32 v242, v240, v28\n"
    "v_exp_f32 v241, v241\n"
    "v_fract_f32 v242, v242\n"
    "v_cos_f32 v212, v242\n"
    "v_sin_f32 v213, v242\n"
    "v_mul_f32 v241, s94, v241\n"
    "v_mul_f32 v212, v212, v241\n"
    "v_mul_f32 v213, v213, v241\n"
    "v_mov_b32 v214, v212\n"
    "v_xor_b32 v215, 0x80000000, v213\n"
    "s_sub_u32 s93, s69, s68\n"
    "s_sub_u32 s93, s93, 5\n"
    "s_lshl_b32 s93, s93, 7\n"
    "s_cmp_lt_i32 s93, 0\n"
    "s_cselect_b32 s94, 0, 1.0\n"
    "s_max_i32 s93, s93, 0\n"
    "v_cvt_f32_i32 v240, s93\n"
    "v_mul_f32 v241, v240, v27\n"
    "v_mul_f32 v242, v240, v28\n"
    "v_exp_f32 v241, v241\n"
    "v_fract_f32 v242, v242\n"
    "v_cos_f32 v216, v242\n"
    "v_sin_f32 v217, v242\n"
    "v_mul_f32 v241, s94, v241\n"
    "v_mul_f32 v216, v216, v241\n"
    "v_mul_f32 v217, v217, v241\n"
    "v_mov_b32 v218, v216\n"
    "v_xor_b32 v219, 0x80000000, v217\n"
    "s_sub_u32 s93, s69, s68\n"
    "s_sub_u32 s93, s93, 9\n"
    "s_lshl_b32 s93, s93, 7\n"
    "s_cmp_lt_i32 s93, 0\n"
    "s_cselect_b32 s94, 0, 1.0\n"
    "s_max_i32 s93, s93, 0\n"
    "v_cvt_f32_i32 v240, s93\n"
    "v_mul_f32 v241, v240, v27\n"
    "v_mul_f32 v242, v240, v28\n"
    "v_exp_f32 v241, v241\n"
    "v_fract_f32 v242, v242\n"
    "v_cos_f32 v220, v242\n"
    "v_sin_f32 v221, v242\n"
    "v_mul_f32 v241, s94, v241\n"
    "v_mul_f32 v220, v220, v241\n"
    "v_mul_f32 v221, v221, v241\n"
    "v_mov_b32 v222, v220\n"
    "v_xor_b32 v223, 0x80000000, v221\n"
    "s_sub_u32 s93, s69, s68\n"
    "s_sub_u32 s93, s93, 13\n"
    "s_lshl_b32 s93, s93, 7\n"
    "s_cmp_lt_i32 s93, 0\n"
    "s_cselect_b32 s94, 0, 1.0\n"
    "s_max_i32 s93, s93, 0\n"
    "v_cvt_f32_i32 v240, s93\n"
    "v_mul_f32 v241, v240, v27\n"
    "v_mul_f32 v242, v240, v28\n"
    "v_exp_f32 v241, v241\n"
    "v_fract_f32 v242, v242\n"
    "v_cos_f32 v224, v242\n"
    "v_sin_f32 v225, v242\n"
    "v_mul_f32 v241, s94, v241\n"
    "v_mul_f32 v224, v224, v241\n"
    "v_mul_f32 v225, v225, v241\n"
    "v_mov_b32 v226, v224\n"
    "v_xor_b32 v227, 0x80000000, v225\n"
    "s_mov_b32 s93, 1\n"
    "v_cvt_f32_i32 v240, s93\n"
    "v_mul_f32 v241, v240, v27\n"
    "v_mul_f32 v242, v240, v28\n"
    "v_exp_f32 v241, v241\n"
    "v_fract_f32 v242, v242\n"
    "v_cos_f32 v228, v242\n"
    "v_sin_f32 v229, v242\n"
    "v_mul_f32 v228, v228, v241\n"
    "v_mul_f32 v229, v229, v241\n"
    "v_mov_b32 v230, v228\n"
    "v_xor_b32 v231, 0x80000000, v229\n"
    "v_add_f32 v197, -1.0, v228\n"
    "v_mul_f32 v240, v24, v24\n"
    "v_fmac_f32 v240, v25, v25\n"
    "v_rcp_f32 v240, v240\n"
    "v_mul_f32 v241, v197, v24\n"
    "v_fmac_f32 v241, v229, v25\n"
    "v_mul_f32 v242, v197, v25\n"
    "v_fma_f32 v242, -v229, v24, v242\n"
    "v_add_f32 v240, v240, v240\n"
    "v_mul_f32 v232, v241, v240\n"
    "v_mul_f32 v233, v242, v240\n"
    "v_xor_b32 v234, 0x80000000, v232\n"
    "v_mov_b32 v235, v233\n"
    "s_mov_b32 exec_hi, 0xff\n"
    "ds_write_b128 v193, v[204:207] offset:0\n"
    "ds_write_b128 v193, v[208:211] offset:2560\n"
    "ds_write_b128 v193, v[212:215] offset:5120\n"
    "ds_write_b128 v193, v[216:219] offset:7680\n"
    "ds_write_b128 v193, v[220:223] offset:10240\n"
    "ds_write_b128 v193, v[224:227] offset:12800\n"
    "s_cmp_eq_u32 s68, 0\n"
    "s_cbranch_scc0 Lk_skip_44\n"
    "ds_write_b128 v193, v[228:231] offset:15360\n"
    "Lk_skip_44:\n"
    "s_cmp_eq_u32 s68, 1\n"
    "s_cbranch_scc0 Lk_skip_45\n"
    "ds_write_b128 v193, v[232:235] offset:15360\n"
    "Lk_skip_45:\n"
    "s_mov_b64 exec, -1\n"
    "v_and_b32 v175, s1, v173\n"
    "v_lshlrev_b32 v174, 16, v173\n"
    "v_and_b32 v173, s1, v172\n"
    "v_lshlrev_b32 v172, 16, v172\n"
    "v_and_b32 v179, s1, v177\n"
    "v_lshlrev_b32 v178, 16, v177\n"
    "v_and_b32 v177, s1, v176\n"
    "v_lshlrev_b32 v176, 16, v176\n"
    "v_and_b32 v183, s1, v181\n"
    "v_lshlrev_b32 v182, 16, v181\n"
    "v_and_b32 v181, s1, v180\n"
    "v_lshlrev_b32 v180, 16, v180\n"
    "v_and_b32 v187, s1, v185\n"
    "v_lshlrev_b32 v186, 16, v185\n"
    "v_and_b32 v185, s1, v184\n"
    "v_lshlrev_b32 v184, 16, v184\n"
    "v_and_b32 v191, s1, v189\n"
    "v_lshlrev_b32 v190, 16, v189\n"
    "v_and_b32 v189, s1, v188\n"
    "v_lshlrev_b32 v188, 16, v188\n"
    "v_pk_add_f32 v[24:25], v[172:173], v[174:175]\n"
    "v_pk_add_f32 v[26:27], v[176:177], v[178:179]\n"
    "v_pk_add_f32 v[24:25], v[24:25], v[180:181]\n"
    "v_pk_add_f32 v[26:27], v[26:27], v[182:183]\n"
    "v_pk_add_f32 v[24:25], v[24:25], v[184:185]\n"
    "v_pk_add_f32 v[26:27], v[26:27], v[186:187]\n"
    "v_pk_add_f32 v[24:25], v[24:25], v[188:189]\n"
    "v_pk_add_f32 v[26:27], v[26:27], v[190:191]\n"
    "v_pk_add_f32 v[24:25], v[24:25], v[26:27]\n"
    "v_add_f32 v24, v24, v25\n"
    "s_nop 1\n"
    "v_add_f32_dpp v24, v24, v24 quad_perm:[1,0,3,2] row_mask:0xf bank_mask:0xf\n"
    "s_nop 1\n"
    "v_add_f32_dpp v24, v24, v24 quad_perm:[2,3,0,1] row_mask:0xf bank_mask:0xf\n"
    "s_nop 1\n"
    "v_add_f32_dpp v24, v24, v24 row_half_mirror row_mask:0xf bank_mask:0xf\n"
    "s_nop 1\n"
    "v_add_f32_dpp v24, v24, v24 row_mirror row_mask:0xf bank_mask:0xf\n"
    "v_mov_b32 v29, v24\n"
    "s_nop 1\n"
    "v_permlane16_swap_b32 v24, v29\n"
    "v_add_f32 v24, v24, v29\n"
    "v_mul_f32 v24, 0x3acccccd, v24\n"
    "v_mov_b32 v25, v24\n"
    "v_pk_add_f32 v[172:173], v[172:173], v[24:25] neg_lo:[0,1] neg_hi:[0,1]\n"
    "v_pk_add_f32 v[174:175], v[174:175], v[24:25] neg_lo:[0,1] neg_hi:[0,1]\n"
    "v_pk_add_f32 v[176:177], v[176:177], v[24:25] neg_lo:[0,1] neg_hi:[0,1]\n"
    "v_pk_add_f32 v[178:179], v[178:179], v[24:25] neg_lo:[0,1] neg_hi:[0,1]\n"
    "v_pk_add_f32 v[180:181], v[180:181], v[24:25] neg_lo:[0,1] neg_hi:[0,1]\n"
    "v_pk_add_f32 v[182:183], v[182:183], v[24:25] neg_lo:[0,1] neg_hi:[0,1]\n"
    "v_pk_add_f32 v[184:185], v[184:185], v[24:25] neg_lo:[0,1] neg_hi:[0,1]\n"
    "v_pk_add_f32 v[186:187], v[186:187], v[24:25] neg_lo:[0,1] neg_hi:[0,1]\n"
    "v_pk_add_f32 v[188:189], v[188:189], v[24:25] neg_lo:[0,1] neg_hi:[0,1]\n"
    "v_pk_add_f32 v[190:191], v[190:191], v[24:25] neg_lo:[0,1] neg_hi:[0,1]\n"
    "v_pk_mul_f32 v[26:27], v[172:173], v[172:173]\n"
    "v_pk_mul_f32 v[30:31], v[174:175], v[174:175]\n"
    "v_pk_fma_f32 v[26:27], v[176:177], v[176:177], v[26:27]\n"
    "v_pk_fma_f32 v[30:31], v[178:179], v[178:179], v[30:31]\n"
    "v_pk_fma_f32 v[26:27], v[180:181], v[180:181], v[26:27]\n"
    "v_pk_fma_f32 v[30:31], v[182:183], v[182:183], v[30:31]\n"
    "v_pk_fma_f32 v[26:27], v[184:185], v[184:185], v[26:27]\n"
    "v_pk_fma_f32 v[30:31], v[186:187], v[186:187], v[30:31]\n"
    "v_pk_fma_f32 v[26:27], v[188:189], v[188:189], v[26:27]\n"
    "v_pk_fma_f32 v[30:31], v[190:191], v[190:191], v[30:31]\n"
    "v_pk_add_f32 v[26:27], v[26:27], v[30:31]\n"
    "v_add_f32 v26, v26, v27\n"
    "s_nop 1\n"
    "v_add_f32_dpp v26, v26, v26 quad_perm:[1,0,3,2] row_mask:0xf bank_mask:0xf\n"
    "s_nop 1\n"
    "v_add_f32_dpp v26, v26, v26 quad_perm:[2,3,0,1] row_mask:0xf bank_mask:0xf\n"
    "s_nop 1\n"
    "v_add_f32_dpp v26, v26, v26 row_half_mirror row_mask:0xf bank_mask:0xf\n"
    "s_nop 1\n"
    "v_add_f32_dpp v26, v26, v26 row_mirror row_mask:0xf bank_mask:0xf\n"
    "v_mov_b32 v29, v26\n"
    "s_nop 1\n"
    "v_permlane16_swap_b32 v26, v29\n"
    "v_add_f32 v26, v26, v29\n"
    "v_mul_f32 v26, 0x3acccccd, v26\n"
    "v_mov_b32 v25, s73\n"
    "v_mov_b32 v27, s73\n"
    "s_mov_b32 s90, 1\n"
    "s_mov_b32 s91, 1\n"
    "s_mov_b64 exec, s[90:91]\n"
    "buffer_store_dwordx4 v[24:27], v192, s[24:27], s3 offen sc1\n"
    "s_mov_b64 exec, -1\n"
    "v_mov_b32 v251, s73\n"
    "s_waitcnt lgkmcnt(0)\n"
    "s_barrier\n"
    "ds_read_b128 v[108:111], v5 offset:0\n"
    "ds_read_b64_tr_b16 v[124:125], v9 offset:0\n"
    "ds_read_b64_tr_b16 v[126:127], v9 offset:768\n"
    "ds_read_b128 v[112:115], v5 offset:512\n"
    "ds_read_b64_tr_b16 v[128:129], v10 offset:32\n"
    "ds_read_b64_tr_b16 v[130:131], v10 offset:800\n"
    "ds_read_b64_tr_b16 v[132:133], v9 offset:64\n"
    "ds_read_b64_tr_b16 v[134:135], v9 offset:832\n"
    "ds_read_b64_tr_b16 v[136:137], v10 offset:96\n"
    "ds_read_b64_tr_b16 v[138:139], v10 offset:864\n"
    "ds_read_b64_tr_b16 v[140:141], v9 offset:128\n"
    "ds_read_b64_tr_b16 v[142:143], v9 offset:896\n"
    "ds_read_b64_tr_b16 v[144:145], v10 offset:160\n"
    "ds_read_b64_tr_b16 v[146:147], v10 offset:928\n"
    "s_waitcnt lgkmcnt(0)\n"
    "s_cmp_eq_u32 s22, 3\n"
    "s_cbranch_scc0 Lk_skip_46\n"
    "ds_read_b128 v[204:207], v244 offset:0\n"
    "ds_read_b128 v[208:211], v244 offset:1024\n"
    "ds_read_b128 v[212:215], v244 offset:2048\n"
    "ds_read_b128 v[216:219], v244 offset:3072\n"
    "ds_read_b128 v[220:223], v244 offset:4096\n"
    "ds_read_b128 v[224:227], v245 offset:0\n"
    "ds_read_b128 v[228:231], v245 offset:1024\n"
    "ds_read_b128 v[232:235], v245 offset:2048\n"
    "ds_read_b128 v[236:239], v245 offset:3072\n"
    "ds_read_b128 v[240:243], v245 offset:4096\n"
    "Lk_skip_46:\n"
    "v_mfma_f32_16x16x32_bf16 v[36:39], v[124:127], v[108:111], 0\n"
    "ds_read_b128 v[116:119], v6 offset:0\n"
    "ds_read_b64_tr_b16 v[148:149], v9 offset:6144\n"
    "v_mfma_f32_16x16x32_bf16 v[40:43], v[124:127], v[112:115], 0\n"
    "ds_read_b64_tr_b16 v[150:151], v9 offset:6912\n"
    "ds_read_b128 v[120:123], v6 offset:512\n"
    "v_mfma_f32_16x16x32_bf16 v[44:47], v[128:131], v[108:111], 0\n"
    "ds_read_b64_tr_b16 v[152:153], v10 offset:6176\n"
    "ds_read_b64_tr_b16 v[154:155], v10 offset:6944\n"
    "v_mfma_f32_16x16x32_bf16 v[48:51], v[128:131], v[112:115], 0\n"
    "ds_read_b64_tr_b16 v[156:157], v9 offset:6208\n"
    "ds_read_b64_tr_b16 v[158:159], v9 offset:6976\n"
    "v_mfma_f32_16x16x32_bf16 v[52:55], v[132:135], v[108:111], 0\n"
    "ds_read_b64_tr_b16 v[160:161], v10 offset:6240\n"
    "ds_read_b64_tr_b16 v[162:163], v10 offset:7008\n"
    "v_mfma_f32_16x16x32_bf16 v[56:59], v[132:135], v[112:115], 0\n"
    "ds_read_b64_tr_b16 v[164:165], v9 offset:6272\n"
    "ds_read_b64_tr_b16 v[166:167], v9 offset:7040\n"
    "v_mfma_f32_16x16x32_bf16 v[60:63], v[136:139], v[108:111], 0\n"
    "ds_read_b64_tr_b16 v[168:169], v10 offset:6304\n"
    "ds_read_b64_tr_b16 v[170:171], v10 offset:7072\n"
    "v_mfma_f32_16x16x32_bf16 v[64:67], v[136:139], v[112:115], 0\n"
    "v_mfma_f32_16x16x32_bf16 v[68:71], v[140:143], v[108:111], 0\n"
    "v_mfma_f32_16x16x32_bf16 v[72:75], v[140:143], v[112:115], 0\n"
    "v_mfma_f32_16x16x32_bf16 v[76:79], v[144:147], v[108:111], 0\n"
    "v_mfma_f32_16x16x32_bf16 v[80:83], v[144:147], v[112:115], 0\n"
    "s_cmp_eq_u32 s22, 2\n"
    "s_cbranch_scc0 Lk_skip_47\n"
    "v_mfma_f32_16x16x32_bf16 v[84:87], v[32:35], v[124:127], 0\n"
    "v_mfma_f32_16x16x32_bf16 v[88:91], v[32:35], v[128:131], 0\n"
    "v_mfma_f32_16x16x32_bf16 v[92:95], v[32:35], v[132:135], 0\n"
    "v_mfma_f32_16x16x32_bf16 v[96:99], v[32:35], v[136:139], 0\n"
    "v_mfma_f32_16x16x32_bf16 v[100:103], v[32:35], v[140:143], 0\n"
    "v_mfma_f32_16x16x32_bf16 v[104:107], v[32:35], v[144:147], 0\n"
    "Lk_skip_47:\n"
    "s_cmp_eq_u32 s22, 3\n"
    "s_cbranch_scc0 Lk_skip_48\n"
    "s_waitcnt lgkmcnt(14)\n"
    "v_mfma_f32_16x16x32_bf16 v[84:87], v[32:35], v[204:207], 0\n"
    "s_waitcnt lgkmcnt(14)\n"
    "v_mfma_f32_16x16x32_bf16 v[88:91], v[32:35], v[208:211], 0\n"
    "s_waitcnt lgkmcnt(14)\n"
    "v_mfma_f32_16x16x32_bf16 v[92:95], v[32:35], v[212:215], 0\n"
    "s_waitcnt lgkmcnt(14)\n"
    "v_mfma_f32_16x16x32_bf16 v[96:99], v[32:35], v[216:219], 0\n"
    "s_waitcnt lgkmcnt(14)\n"
    "v_mfma_f32_16x16x32_bf16 v[100:103], v[32:35], v[220:223], 0\n"
    "Lk_skip_48:\n"
    "s_waitcnt lgkmcnt(0)\n"
    "s_barrier\n"
    "s_setprio 1\n"
    "v_mfma_f32_16x16x32_bf16 v[36:39], v[148:151], v[116:119], v[36:39]\n"
    "s_add_u32 m0, s5, 116736\n"
    "s_nop 0\n"
    "buffer_load_dwordx4 v248, s[80:83], s4 offen lds\n"
    "ds_read_b128 v[108:111], v5 offset:38912\n"
    "ds_read_b64_tr_b16 v[124:125], v9 offset:38912\n"
    "v_mfma_f32_16x16x32_bf16 v[40:43], v[148:151], v[120:123], v[40:43]\n"
    "ds_read_b64_tr_b16 v[126:127], v9 offset:39680\n"
    "ds_read_b128 v[112:115], v5 offset:39424\n"
    "v_mfma_f32_16x16x32_bf16 v[44:47], v[152:155], v[116:119], v[44:47]\n"
    "s_add_u32 m0, s5, 117760\n"
    "s_nop 0\n"
    "buffer_load_dwordx4 v248, s[80:83], s23 offen lds\n"
    "ds_read_b64_tr_b16 v[128:129], v10 offset:38944\n"
    "ds_read_b64_tr_b16 v[130:131], v10 offset:39712\n"
    "v_mfma_f32_16x16x32_bf16 v[48:51], v[152:155], v[120:123], v[48:51]\n"
    "ds_read_b64_tr_b16 v[132:133], v9 offset:38976\n"
    "ds_read_b64_tr_b16 v[134:135], v9 offset:39744\n"
    "v_mfma_f32_16x16x32_bf16 v[52:55], v[156:159], v[116:119], v[52:55]\n"
    "ds_read_b64_tr_b16 v[136:137], v10 offset:39008\n"
    "s_add_u32 m0, s5, 118784\n"
    "s_nop 0\n"
    "buffer_load_dwordx4 v248, s[84:87], s39 offen lds\n"
    "ds_read_b64_tr_b16 v[138:139], v10 offset:39776\n"
    "v_mfma_f32_16x16x32_bf16 v[56:59], v[156:159], v[120:123], v[56:59]\n"
    "ds_read_b64_tr_b16 v[140:141], v9 offset:39040\n"
    "ds_read_b64_tr_b16 v[142:143], v9 offset:39808\n"
    "v_mfma_f32_16x16x32_bf16 v[60:63], v[160:163], v[116:119], v[60:63]\n"
    "ds_read_b64_tr_b16 v[144:145], v10 offset:39072\n"
    "s_add_u32 m0, s5, 119808\n"
    "s_nop 0\n"
    "buffer_load_dwordx4 v248, s[84:87], s55 offen lds\n"
    "ds_read_b64_tr_b16 v[146:147], v10 offset:39840\n"
    "v_mfma_f32_16x16x32_bf16 v[64:67], v[160:163], v[120:123], v[64:67]\n"
    "v_mfma_f32_16x16x32_bf16 v[68:71], v[164:167], v[116:119], v[68:71]\n"
    "v_mfma_f32_16x16x32_bf16 v[72:75], v[164:167], v[120:123], v[72:75]\n"
    "v_mfma_f32_16x16x32_bf16 v[76:79], v[168:171], v[116:119], v[76:79]\n"
    "v_mfma_f32_16x16x32_bf16 v[80:83], v[168:171], v[120:123], v[80:83]\n"
    "s_setprio 0\n"
    "s_cmp_eq_u32 s22, 2\n"
    "s_cbranch_scc0 Lk_skip_49\n"
    "v_mfma_f32_16x16x32_bf16 v[84:87], v[32:35], v[148:151], v[84:87]\n"
    "v_mfma_f32_16x16x32_bf16 v[88:91], v[32:35], v[152:155], v[88:91]\n"
    "v_mfma_f32_16x16x32_bf16 v[92:95], v[32:35], v[156:159], v[92:95]\n"
    "v_mfma_f32_16x16x32_bf16 v[96:99], v[32:35], v[160:163], v[96:99]\n"
    "v_mfma_f32_16x16x32_bf16 v[100:103], v[32:35], v[164:167], v[100:103]\n"
    "v_mfma_f32_16x16x32_bf16 v[104:107], v[32:35], v[168:171], v[104:107]\n"
    "Lk_skip_49:\n"
    "s_cmp_eq_u32 s22, 3\n"
    "s_cbranch_scc0 Lk_skip_50\n"
    "v_mfma_f32_16x16x32_bf16 v[84:87], v[32:35], v[224:227], v[84:87]\n"
    "v_mfma_f32_16x16x32_bf16 v[88:91], v[32:35], v[228:231], v[88:91]\n"
    "v_mfma_f32_16x16x32_bf16 v[92:95], v[32:35], v[232:235], v[92:95]\n"
    "v_mfma_f32_16x16x32_bf16 v[96:99], v[32:35], v[236:239], v[96:99]\n"
    "v_mfma_f32_16x16x32_bf16 v[100:103], v[32:35], v[240:243], v[100:103]\n"
    "Lk_skip_50:\n"
    "s_waitcnt lgkmcnt(0)\n"
    "s_cmp_eq_u32 s22, 7\n"
    "s_cbranch_scc0 Lk_skip_51\n"
    "ds_read_b128 v[204:207], v244 offset:38912\n"
    "ds_read_b128 v[208:211], v244 offset:39936\n"
    "ds_read_b128 v[212:215], v244 offset:40960\n"
    "ds_read_b128 v[216:219], v244 offset:41984\n"
    "ds_read_b128 v[220:223], v244 offset:43008\n"
    "ds_read_b128 v[224:227], v245 offset:38912\n"
    "ds_read_b128 v[228:231], v245 offset:39936\n"
    "ds_read_b128 v[232:235], v245 offset:40960\n"
    "ds_read_b128 v[236:239], v245 offset:41984\n"
    "ds_read_b128 v[240:243], v245 offset:43008\n"
    "Lk_skip_51:\n"
    "v_mfma_f32_16x16x32_bf16 v[36:39], v[124:127], v[108:111], v[36:39]\n"
    "v_min_u32 v29, 11, v1\n"
    "ds_read_b128 v[116:119], v6 offset:38912\n"
    "ds_read_b64_tr_b16 v[148:149], v9 offset:45056\n"
    "s_mul_i32 s92, s68, 12\n"
    "v_mfma_f32_16x16x32_bf16 v[40:43], v[124:127], v[112:115], v[40:43]\n"
    "ds_read_b64_tr_b16 v[150:151], v9 offset:45824\n"
    "ds_read_b128 v[120:123], v6 offset:39424\n"
    "s_add_u32 s92, s92, 40\n"
    "v_mfma_f32_16x16x32_bf16 v[44:47], v[128:131], v[108:111], v[44:47]\n"
    "ds_read_b64_tr_b16 v[152:153], v10 offset:45088\n"
    "ds_read_b64_tr_b16 v[154:155], v10 offset:45856\n"
    "v_add_u32 v29, s92, v29\n"
    "v_mfma_f32_16x16x32_bf16 v[48:51], v[128:131], v[112:115], v[48:51]\n"
    "ds_read_b64_tr_b16 v[156:157], v9 offset:45120\n"
    "ds_read_b64_tr_b16 v[158:159], v9 offset:45888\n"
    "v_lshlrev_b32 v196, 5, v29\n"
    "v_mfma_f32_16x16x32_bf16 v[52:55], v[132:135], v[108:111], v[52:55]\n"
    "ds_read_b64_tr_b16 v[160:161], v10 offset:45152\n"
    "ds_read_b64_tr_b16 v[162:163], v10 offset:45920\n"
    "v_add_u32 v196, 133120, v196\n"
    "v_mfma_f32_16x16x32_bf16 v[56:59], v[132:135], v[112:115], v[56:59]\n"
    "ds_read_b64_tr_b16 v[164:165], v9 offset:45184\n"
    "ds_read_b64_tr_b16 v[166:167], v9 offset:45952\n"
    "v_mul_u32_u24 v30, 1408, v2\n"
    "v_mfma_f32_16x16x32_bf16 v[60:63], v[136:139], v[108:111], v[60:63]\n"
    "ds_read_b64_tr_b16 v[168:169], v10 offset:45216\n"
    "ds_read_b64_tr_b16 v[170:171], v10 offset:45984\n"
    "v_lshl_add_u32 v194, v29, 4, v30\n"
    "v_mfma_f32_16x16x32_bf16 v[64:67], v[136:139], v[112:115], v[64:67]\n"
    "v_mfma_f32_16x16x32_bf16 v[68:71], v[140:143], v[108:111], v[68:71]\n"
    "v_mfma_f32_16x16x32_bf16 v[72:75], v[140:143], v[112:115], v[72:75]\n"
    "v_mov_b32 v30, 0x40000000\n"
    "v_mfma_f32_16x16x32_bf16 v[76:79], v[144:147], v[108:111], v[76:79]\n"
    "v_mfma_f32_16x16x32_bf16 v[80:83], v[144:147], v[112:115], v[80:83]\n"
    "s_cmp_eq_u32 s22, 6\n"
    "s_cbranch_scc0 Lk_skip_52\n"
    "v_mfma_f32_16x16x32_bf16 v[84:87], v[32:35], v[124:127], 0\n"
    "v_mfma_f32_16x16x32_bf16 v[88:91], v[32:35], v[128:131], 0\n"
    "v_mfma_f32_16x16x32_bf16 v[92:95], v[32:35], v[132:135], 0\n"
    "v_mfma_f32_16x16x32_bf16 v[96:99], v[32:35], v[136:139], 0\n"
    "v_mfma_f32_16x16x32_bf16 v[100:103], v[32:35], v[140:143], 0\n"
    "v_mfma_f32_16x16x32_bf16 v[104:107], v[32:35], v[144:147], 0\n"
    "Lk_skip_52:\n"
    "s_cmp_eq_u32 s22, 7\n"
    "s_cbranch_scc0 Lk_skip_53\n"
    "s_waitcnt lgkmcnt(14)\n"
    "v_mfma_f32_16x16x32_bf16 v[84:87], v[32:35], v[204:207], 0\n"
    "s_waitcnt lgkmcnt(14)\n"
    "v_mfma_f32_16x16x32_bf16 v[88:91], v[32:35], v[208:211], 0\n"
    "s_waitcnt lgkmcnt(14)\n"
    "v_mfma_f32_16x16x32_bf16 v[92:95], v[32:35], v[212:215], 0\n"
    "s_waitcnt lgkmcnt(14)\n"
    "v_mfma_f32_16x16x32_bf16 v[96:99], v[32:35], v[216:219], 0\n"
    "s_waitcnt lgkmcnt(14)\n"
    "v_mfma_f32_16x16x32_bf16 v[100:103], v[32:35], v[220:223], 0\n"
    "Lk_skip_53:\n"
    "s_waitcnt lgkmcnt(0)\n"
    "s_barrier\n"
    "v_mfma_f32_16x16x32_bf16 v[36:39], v[148:151], v[116:119], v[36:39]\n"
    "v_cmp_gt_u32 vcc, 32, v0\n"
    "s_nop 1\n"
    "v_cndmask_b32 v195, v30, v194, vcc\n"
    "ds_read_b128 v[108:111], v7 offset:0\n"
    "ds_read_b64_tr_b16 v[124:125], v11 offset:0\n"
    "s_add_u32 s18, s17, 5632\n"
    "v_mfma_f32_16x16x32_bf16 v[40:43], v[148:151], v[120:123], v[40:43]\n"
    "ds_read_b64_tr_b16 v[126:127], v11 offset:768\n"
    "ds_read_b128 v[112:115], v7 offset:512\n"
    "s_add_u32 s35, s17, 11264\n"
    "v_mfma_f32_16x16x32_bf16 v[44:47], v[152:155], v[116:119], v[44:47]\n"
    "ds_read_b64_tr_b16 v[128:129], v12 offset:32\n"
    "ds_read_b64_tr_b16 v[130:131], v12 offset:800\n"
    "s_mov_b64 s[36:37], 0xffffffff\n"
    "v_mfma_f32_16x16x32_bf16 v[48:51], v[152:155], v[120:123], v[48:51]\n"
    "ds_read_b64_tr_b16 v[132:133], v11 offset:64\n"
    "ds_read_b64_tr_b16 v[134:135], v11 offset:832\n"
    "s_lshl_b32 s92, s68, 7\n"
    "v_mfma_f32_16x16x32_bf16 v[52:55], v[156:159], v[116:119], v[52:55]\n"
    "ds_read_b64_tr_b16 v[136:137], v12 offset:96\n"
    "ds_read_b64_tr_b16 v[138:139], v12 offset:864\n"
    "s_add_u32 s92, s92, 158976\n"
    "v_mfma_f32_16x16x32_bf16 v[56:59], v[156:159], v[120:123], v[56:59]\n"
    "ds_read_b64_tr_b16 v[140:141], v11 offset:128\n"
    "ds_read_b64_tr_b16 v[142:143], v11 offset:896\n"
    "v_lshl_add_u32 v14, v1, 2, s92\n"
    "v_mfma_f32_16x16x32_bf16 v[60:63], v[160:163], v[116:119], v[60:63]\n"
    "ds_read_b64_tr_b16 v[144:145], v12 offset:160\n"
    "ds_read_b64_tr_b16 v[146:147], v12 offset:928\n"
    "v_lshlrev_b32 v15, 4, v1\n"
    "v_mfma_f32_16x16x32_bf16 v[64:67], v[160:163], v[120:123], v[64:67]\n"
    "v_mfma_f32_16x16x32_bf16 v[68:71], v[164:167], v[116:119], v[68:71]\n"
    "v_mfma_f32_16x16x32_bf16 v[72:75], v[164:167], v[120:123], v[72:75]\n"
    "v_add_u32 v15, 134400, v15\n"
    "v_mfma_f32_16x16x32_bf16 v[76:79], v[168:171], v[116:119], v[76:79]\n"
    "v_mfma_f32_16x16x32_bf16 v[80:83], v[168:171], v[120:123], v[80:83]\n"
    "s_cmp_eq_u32 s22, 6\n"
    "s_cbranch_scc0 Lk_skip_54\n"
    "v_mfma_f32_16x16x32_bf16 v[84:87], v[32:35], v[148:151], v[84:87]\n"
    "v_mfma_f32_16x16x32_bf16 v[88:91], v[32:35], v[152:155], v[88:91]\n"
    "v_mfma_f32_16x16x32_bf16 v[92:95], v[32:35], v[156:159], v[92:95]\n"
    "v_mfma_f32_16x16x32_bf16 v[96:99], v[32:35], v[160:163], v[96:99]\n"
    "v_mfma_f32_16x16x32_bf16 v[100:103], v[32:35], v[164:167], v[100:103]\n"
    "v_mfma_f32_16x16x32_bf16 v[104:107], v[32:35], v[168:171], v[104:107]\n"
    "Lk_skip_54:\n"
    "s_cmp_eq_u32 s22, 7\n"
    "s_cbranch_scc0 Lk_skip_55\n"
    "v_mfma_f32_16x16x32_bf16 v[84:87], v[32:35], v[224:227], v[84:87]\n"
    "v_mfma_f32_16x16x32_bf16 v[88:91], v[32:35], v[228:231], v[88:91]\n"
    "v_mfma_f32_16x16x32_bf16 v[92:95], v[32:35], v[232:235], v[92:95]\n"
    "v_mfma_f32_16x16x32_bf16 v[96:99], v[32:35], v[236:239], v[96:99]\n"
    "v_mfma_f32_16x16x32_bf16 v[100:103], v[32:35], v[240:243], v[100:103]\n"
    "Lk_skip_55:\n"
    "s_waitcnt lgkmcnt(0)\n"
    "s_cmp_eq_u32 s22, 11\n"
    "s_cbranch_scc0 Lk_skip_56\n"
    "ds_read_b128 v[204:207], v246 offset:0\n"
    "ds_read_b128 v[208:211], v246 offset:1024\n"
    "ds_read_b128 v[212:215], v246 offset:2048\n"
    "ds_read_b128 v[216:219], v246 offset:3072\n"
    "ds_read_b128 v[220:223], v246 offset:4096\n"
    "ds_read_b128 v[224:227], v247 offset:0\n"
    "ds_read_b128 v[228:231], v247 offset:1024\n"
    "ds_read_b128 v[232:235], v247 offset:2048\n"
    "ds_read_b128 v[236:239], v247 offset:3072\n"
    "ds_read_b128 v[240:243], v247 offset:4096\n"
    "Lk_skip_56:\n"
    "s_waitcnt vmcnt(0)\n"
    "s_cmp_lt_u32 s68, 2\n"
    "s_cbranch_scc0 Lk_skip_57\n"
    "ds_read_b64_tr_b16 v[172:173], v200 offset:0\n"
    "ds_read_b64_tr_b16 v[174:175], v200 offset:2048\n"
    "ds_read_b64_tr_b16 v[176:177], v200 offset:32\n"
    "ds_read_b64_tr_b16 v[178:179], v200 offset:2080\n"
    "ds_read_b64_tr_b16 v[180:181], v200 offset:64\n"
    "ds_read_b64_tr_b16 v[182:183], v200 offset:2112\n"
    "ds_read_b64_tr_b16 v[184:185], v200 offset:96\n"
    "ds_read_b64_tr_b16 v[186:187], v200 offset:2144\n"
    "Lk_skip_57:\n"
    "s_cmp_ge_u32 s68, 2\n"
    "s_cbranch_scc0 Lk_skip_58\n"
    "s_waitcnt lgkmcnt(0)\n"
    "ds_read_b128 v[172:175], v200 offset:0\n"
    "s_waitcnt lgkmcnt(0)\n"
    "ds_read_b128 v[176:179], v200 offset:64\n"
    "s_waitcnt lgkmcnt(0)\n"
    "ds_read_b128 v[180:183], v200 offset:2048\n"
    "s_waitcnt lgkmcnt(0)\n"
    "ds_read_b128 v[184:187], v200 offset:2112\n"
    "Lk_skip_58:\n"
    "v_mfma_f32_16x16x32_bf16 v[36:39], v[124:127], v[108:111], v[36:39]\n"
    "s_lshl_b32 s92, s68, 9\n"
    "ds_read_b128 v[116:119], v8 offset:0\n"
    "ds_read_b64_tr_b16 v[148:149], v11 offset:6144\n"
    "v_lshl_add_u32 v16, v1, 4, s92\n"
    "v_mfma_f32_16x16x32_bf16 v[40:43], v[124:127], v[112:115], v[40:43]\n"
    "ds_read_b64_tr_b16 v[150:151], v11 offset:6912\n"
    "ds_read_b128 v[120:123], v8 offset:512\n"
    "v_mov_b32 v29, 2048\n"
    "v_mfma_f32_16x16x32_bf16 v[44:47], v[128:131], v[108:111], v[44:47]\n"
    "ds_read_b64_tr_b16 v[152:153], v12 offset:6176\n"
    "ds_read_b64_tr_b16 v[154:155], v12 offset:6944\n"
    "v_cmp_gt_u32 vcc, 16, v0\n"
    "s_nop 1\n"
    "v_cndmask_b32 v16, v29, v16, vcc\n"
    "v_mfma_f32_16x16x32_bf16 v[48:51], v[128:131], v[112:115], v[48:51]\n"
    "ds_read_b64_tr_b16 v[156:157], v11 offset:6208\n"
    "ds_read_b64_tr_b16 v[158:159], v11 offset:6976\n"
    "v_add_u32 v16, 135936, v16\n"
    "v_mfma_f32_16x16x32_bf16 v[52:55], v[132:135], v[108:111], v[52:55]\n"
    "ds_read_b64_tr_b16 v[160:161], v12 offset:6240\n"
    "ds_read_b64_tr_b16 v[162:163], v12 offset:7008\n"
    "s_lshl_b32 s95, s79, 2\n"
    "v_mfma_f32_16x16x32_bf16 v[56:59], v[132:135], v[112:115], v[56:59]\n"
    "ds_read_b64_tr_b16 v[164:165], v11 offset:6272\n"
    "ds_read_b64_tr_b16 v[166:167], v11 offset:7040\n"
    "s_add_u32 s95, s95, s68\n"
    "v_mfma_f32_16x16x32_bf16 v[60:63], v[136:139], v[108:111], v[60:63]\n"
    "ds_read_b64_tr_b16 v[168:169], v12 offset:6304\n"
    "ds_read_b64_tr_b16 v[170:171], v12 offset:7072\n"
    "s_mul_i32 s92, s72, 5\n"
    "v_mfma_f32_16x16x32_bf16 v[64:67], v[136:139], v[112:115], v[64:67]\n"
    "v_mfma_f32_16x16x32_bf16 v[68:71], v[140:143], v[108:111], v[68:71]\n"
    "v_mfma_f32_16x16x32_bf16 v[72:75], v[140:143], v[112:115], v[72:75]\n"
    "s_lshr_b32 s93, s92, 2\n"
    "v_mfma_f32_16x16x32_bf16 v[76:79], v[144:147], v[108:111], v[76:79]\n"
    "v_mfma_f32_16x16x32_bf16 v[80:83], v[144:147], v[112:115], v[80:83]\n"
    "s_cmp_eq_u32 s22, 10\n"
    "s_cbranch_scc0 Lk_skip_59\n"
    "v_mfma_f32_16x16x32_bf16 v[84:87], v[32:35], v[124:127], 0\n"
    "v_mfma_f32_16x16x32_bf16 v[88:91], v[32:35], v[128:131], 0\n"
    "v_mfma_f32_16x16x32_bf16 v[92:95], v[32:35], v[132:135], 0\n"
    "v_mfma_f32_16x16x32_bf16 v[96:99], v[32:35], v[136:139], 0\n"
    "v_mfma_f32_16x16x32_bf16 v[100:103], v[32:35], v[140:143], 0\n"
    "v_mfma_f32_16x16x32_bf16 v[104:107], v[32:35], v[144:147], 0\n"
    "Lk_skip_59:\n"
    "s_cmp_eq_u32 s22, 11\n"
    "s_cbranch_scc0 Lk_skip_60\n"
    "s_waitcnt lgkmcnt(14)\n"
    "v_mfma_f32_16x16x32_bf16 v[84:87], v[32:35], v[204:207], 0\n"
    "s_waitcnt lgkmcnt(14)\n"
    "v_mfma_f32_16x16x32_bf16 v[88:91], v[32:35], v[208:211], 0\n"
    "s_waitcnt lgkmcnt(14)\n"
    "v_mfma_f32_16x16x32_bf16 v[92:95], v[32:35], v[212:215], 0\n"
    "s_waitcnt lgkmcnt(14)\n"
    "v_mfma_f32_16x16x32_bf16 v[96:99], v[32:35], v[216:219], 0\n"
    "s_waitcnt lgkmcnt(14)\n"
    "v_mfma_f32_16x16x32_bf16 v[100:103], v[32:35], v[220:223], 0\n"
    "Lk_skip_60:\n"
    "s_waitcnt lgkmcnt(0)\n"
    "s_barrier\n"
    "s_setprio 1\n"
    "v_mfma_f32_16x16x32_bf16 v[36:39], v[148:151], v[116:119], v[36:39]\n"
    "s_mul_i32 s93, s93, 12\n"
    "buffer_store_dwordx4 v[172:175], v201, s[24:27], s12 offen sc1\n"
    "ds_read_b128 v[108:111], v5 offset:0\n"
    "ds_read_b64_tr_b16 v[124:125], v9 offset:0\n"
    "s_lshr_b32 s94, s95, 1\n"
    "v_mfma_f32_16x16x32_bf16 v[40:43], v[148:151], v[120:123], v[40:43]\n"
    "ds_read_b64_tr_b16 v[126:127], v9 offset:768\n"
    "ds_read_b128 v[112:115], v5 offset:512\n"
    "s_mul_i32 s94, s94, 120\n"
    "v_mfma_f32_16x16x32_bf16 v[44:47], v[152:155], v[116:119], v[44:47]\n"
    "buffer_store_dwordx4 v[176:179], v201, s[24:27], s13 offen sc1\n"
    "ds_read_b64_tr_b16 v[128:129], v10 offset:32\n"
    "ds_read_b64_tr_b16 v[130:131], v10 offset:800\n"
    "s_add_u32 s93, s93, s94\n"
    "v_mfma_f32_16x16x32_bf16 v[48:51], v[152:155], v[120:123], v[48:51]\n"
    "ds_read_b64_tr_b16 v[132:133], v9 offset:64\n"
    "ds_read_b64_tr_b16 v[134:135], v9 offset:832\n"
    "s_and_b32 s94, s68, 1\n"
    "v_mfma_f32_16x16x32_bf16 v[52:55], v[156:159], v[116:119], v[52:55]\n"
    "ds_read_b64_tr_b16 v[136:137], v10 offset:96\n"
    "buffer_store_dwordx4 v[180:183], v201, s[24:27], s14 offen sc1\n"
    "ds_read_b64_tr_b16 v[138:139], v10 offset:864\n"
    "s_mul_i32 s94, s94, 6\n"
    "v_mfma_f32_16x16x32_bf16 v[56:59], v[156:159], v[120:123], v[56:59]\n"
    "ds_read_b64_tr_b16 v[140:141], v9 offset:128\n"
    "ds_read_b64_tr_b16 v[142:143], v9 offset:896\n"
    "s_add_u32 s93, s93, s94\n"
    "v_mfma_f32_16x16x32_bf16 v[60:63], v[160:163], v[116:119], v[60:63]\n"
    "ds_read_b64_tr_b16 v[144:145], v10 offset:160\n"
    "buffer_store_dwordx4 v[184:187], v201, s[24:27], s15 offen sc1\n"
    "ds_read_b64_tr_b16 v[146:147], v10 offset:928\n"
    "s_add_u32 s93, s93, 256\n"
    "v_mfma_f32_16x16x32_bf16 v[64:67], v[160:163], v[120:123], v[64:67]\n"
    "v_mfma_f32_16x16x32_bf16 v[68:71], v[164:167], v[116:119], v[68:71]\n"
    "v_mfma_f32_16x16x32_bf16 v[72:75], v[164:167], v[120:123], v[72:75]\n"
    "v_min_u32 v29, 11, v0\n"
    "v_mfma_f32_16x16x32_bf16 v[76:79], v[168:171], v[116:119], v[76:79]\n"
    "v_mfma_f32_16x16x32_bf16 v[80:83], v[168:171], v[120:123], v[80:83]\n"
    "s_setprio 0\n"
    "s_cmp_eq_u32 s22, 10\n"
    "s_cbranch_scc0 Lk_skip_61\n"
    "v_mfma_f32_16x16x32_bf16 v[84:87], v[32:35], v[148:151], v[84:87]\n"
    "v_mfma_f32_16x16x32_bf16 v[88:91], v[32:35], v[152:155], v[88:91]\n"
    "v_mfma_f32_16x16x32_bf16 v[92:95], v[32:35], v[156:159], v[92:95]\n"
    "v_mfma_f32_16x16x32_bf16 v[96:99], v[32:35], v[160:163], v[96:99]\n"
    "v_mfma_f32_16x16x32_bf16 v[100:103], v[32:35], v[164:167], v[100:103]\n"
    "v_mfma_f32_16x16x32_bf16 v[104:107], v[32:35], v[168:171], v[104:107]\n"
    "Lk_skip_61:\n"
    "s_cmp_eq_u32 s22, 11\n"
    "s_cbranch_scc0 Lk_skip_62\n"
    "v_mfma_f32_16x16x32_bf16 v[84:87], v[32:35], v[224:227], v[84:87]\n"
    "v_mfma_f32_16x16x32_bf16 v[88:91], v[32:35], v[228:231], v[88:91]\n"
    "v_mfma_f32_16x16x32_bf16 v[92:95], v[32:35], v[232:235], v[92:95]\n"
    "v_mfma_f32_16x16x32_bf16 v[96:99], v[32:35], v[236:239], v[96:99]\n"
    "v_mfma_f32_16x16x32_bf16 v[100:103], v[32:35], v[240:243], v[100:103]\n"
    "Lk_skip_62:\n"
    "s_waitcnt lgkmcnt(0)\n"
    "s_setprio 1\n"
    "v_mfma_f32_16x16x32_bf16 v[36:39], v[124:127], v[108:111], v[36:39]\n"
    "v_add_u32 v30, 6, v29\n"
    "s_cmp_lt_u32 s16, 0x40000000\n"
    "s_cbranch_scc0 Lk_skip_63\n"
    "v_mov_b32 v85, s73\n"
    "buffer_store_dwordx2 v[84:85], v249, s[24:27], s16 offen offset:0 sc1\n"
    "v_mov_b32 v89, s73\n"
    "buffer_store_dwordx2 v[88:89], v249, s[24:27], s16 offen offset:128 sc1\n"
    "v_mov_b32 v93, s73\n"
    "buffer_store_dwordx2 v[92:93], v249, s[24:27], s16 offen offset:256 sc1\n"
    "v_mov_b32 v97, s73\n"
    "buffer_store_dwordx2 v[96:97], v249, s[24:27], s16 offen offset:384 sc1\n"
    "v_mov_b32 v101, s73\n"
    "buffer_store_dwordx2 v[100:101], v249, s[24:27], s16 offen offset:512 sc1\n"
    "v_mov_b32 v105, s73\n"
    "buffer_store_dwordx2 v[104:105], v249, s[24:27], s88 offen offset:640 sc1\n"
    "s_nop 9\n"
    "Lk_skip_63:\n"
    "ds_read_b128 v[116:119], v6 offset:0\n"
    "ds_read_b64_tr_b16 v[148:149], v9 offset:6144\n"
    "v_cmp_lt_u32 vcc, 5, v29\n"
    "s_nop 1\n"
    "v_cndmask_b32 v29, v29, v30, vcc\n"
    "v_mfma_f32_16x16x32_bf16 v[40:43], v[124:127], v[112:115], v[40:43]\n"
    "ds_read_b64_tr_b16 v[150:151], v9 offset:6912\n"
    "ds_read_b128 v[120:123], v6 offset:512\n"
    "v_add_u32 v29, s93, v29\n"
    "v_mfma_f32_16x16x32_bf16 v[44:47], v[128:131], v[108:111], v[44:47]\n"
    "ds_read_b64_tr_b16 v[152:153], v10 offset:6176\n"
    "ds_read_b64_tr_b16 v[154:155], v10 offset:6944\n"
    "s_mul_i32 s92, s92, 10\n"
    "v_mfma_f32_16x16x32_bf16 v[48:51], v[128:131], v[112:115], v[48:51]\n"
    "ds_read_b64_tr_b16 v[156:157], v9 offset:6208\n"
    "ds_read_b64_tr_b16 v[158:159], v9 offset:6976\n"
    "s_mul_i32 s94, s95, 5\n"
    "v_mfma_f32_16x16x32_bf16 v[52:55], v[132:135], v[108:111], v[52:55]\n"
    "ds_read_b64_tr_b16 v[160:161], v10 offset:6240\n"
    "ds_read_b64_tr_b16 v[162:163], v10 offset:7008\n"
    "s_lshr_b32 s94, s94, 2\n"
    "v_mfma_f32_16x16x32_bf16 v[56:59], v[132:135], v[112:115], v[56:59]\n"
    "ds_read_b64_tr_b16 v[164:165], v9 offset:6272\n"
    "ds_read_b64_tr_b16 v[166:167], v9 offset:7040\n"
    "s_add_u32 s92, s92, s94\n"
    "v_mfma_f32_16x16x32_bf16 v[60:63], v[136:139], v[108:111], v[60:63]\n"
    "ds_read_b64_tr_b16 v[168:169], v10 offset:6304\n"
    "ds_read_b64_tr_b16 v[170:171], v10 offset:7072\n"
    "s_add_u32 s92, s92, 768\n"
    "v_mfma_f32_16x16x32_bf16 v[64:67], v[136:139], v[112:115], v[64:67]\n"
    "v_mfma_f32_16x16x32_bf16 v[68:71], v[140:143], v[108:111], v[68:71]\n"
    "v_mfma_f32_16x16x32_bf16 v[72:75], v[140:143], v[112:115], v[72:75]\n"
    "v_and_b32 v28, 31, v0\n"
    "v_mfma_f32_16x16x32_bf16 v[76:79], v[144:147], v[108:111], v[76:79]\n"
    "v_mfma_f32_16x16x32_bf16 v[80:83], v[144:147], v[112:115], v[80:83]\n"
    "s_setprio 0\n"
    "s_waitcnt lgkmcnt(0)\n"
    "s_barrier\n"
    "v_mfma_f32_16x16x32_bf16 v[36:39], v[148:151], v[116:119], v[36:39]\n"
    "v_min_u32 v30, 9, v28\n"
    "ds_read_b128 v[108:111], v5 offset:38912\n"
    "ds_read_b64_tr_b16 v[124:125], v9 offset:38912\n"
    "v_mfma_f32_16x16x32_bf16 v[40:43], v[148:151], v[120:123], v[40:43]\n"
    "ds_read_b64_tr_b16 v[126:127], v9 offset:39680\n"
    "v_lshrrev_b32 v31, 1, v30\n"
    "ds_read_b128 v[112:115], v5 offset:39424\n"
    "v_mfma_f32_16x16x32_bf16 v[44:47], v[152:155], v[116:119], v[44:47]\n"
    "ds_read_b64_tr_b16 v[128:129], v10 offset:38944\n"
    "ds_read_b64_tr_b16 v[130:131], v10 offset:39712\n"
    "v_lshl_add_u32 v30, v31, 3, v30\n"
    "v_mfma_f32_16x16x32_bf16 v[48:51], v[152:155], v[120:123], v[48:51]\n"
    "ds_read_b64_tr_b16 v[132:133], v9 offset:38976\n"
    "ds_read_b64_tr_b16 v[134:135], v9 offset:39744\n"
    "v_mfma_f32_16x16x32_bf16 v[52:55], v[156:159], v[116:119], v[52:55]\n"
    "ds_read_b64_tr_b16 v[136:137], v10 offset:39008\n"
    "v_add_u32 v30, s92, v30\n"
    "ds_read_b64_tr_b16 v[138:139], v10 offset:39776\n"
    "v_mfma_f32_16x16x32_bf16 v[56:59], v[156:159], v[120:123], v[56:59]\n"
    "ds_read_b64_tr_b16 v[140:141], v9 offset:39040\n"
    "ds_read_b64_tr_b16 v[142:143], v9 offset:39808\n"
    "v_cmp_gt_u32 vcc, 32, v0\n"
    "s_nop 1\n"
    "v_cndmask_b32 v29, v30, v29, vcc\n"
    "v_mfma_f32_16x16x32_bf16 v[60:63], v[160:163], v[116:119], v[60:63]\n"
    "ds_read_b64_tr_b16 v[144:145], v10 offset:39072\n"
    "ds_read_b64_tr_b16 v[146:147], v10 offset:39840\n"
    "v_mfma_f32_16x16x32_bf16 v[64:67], v[160:163], v[120:123], v[64:67]\n"
    "v_lshlrev_b32 v198, 2, v29\n"
    "v_mfma_f32_16x16x32_bf16 v[68:71], v[164:167], v[116:119], v[68:71]\n"
    "v_mfma_f32_16x16x32_bf16 v[72:75], v[164:167], v[120:123], v[72:75]\n"
    "v_mfma_f32_16x16x32_bf16 v[76:79], v[168:171], v[116:119], v[76:79]\n"
    "v_mfma_f32_16x16x32_bf16 v[80:83], v[168:171], v[120:123], v[80:83]\n"
    "s_waitcnt lgkmcnt(0)\n"
    "v_mfma_f32_16x16x32_bf16 v[36:39], v[124:127], v[108:111], v[36:39]\n"
    "ds_read_b128 v[116:119], v6 offset:38912\n"
    "ds_read_b64_tr_b16 v[148:149], v9 offset:45056\n"
    "v_mfma_f32_16x16x32_bf16 v[40:43], v[124:127], v[112:115], v[40:43]\n"
    "ds_read_b64_tr_b16 v[150:151], v9 offset:45824\n"
    "ds_read_b128 v[120:123], v6 offset:39424\n"
    "v_mfma_f32_16x16x32_bf16 v[44:47], v[128:131], v[108:111], v[44:47]\n"
    "ds_read_b64_tr_b16 v[152:153], v10 offset:45088\n"
    "ds_read_b64_tr_b16 v[154:155], v10 offset:45856\n"
    "v_mfma_f32_16x16x32_bf16 v[48:51], v[128:131], v[112:115], v[48:51]\n"
    "ds_read_b64_tr_b16 v[156:157], v9 offset:45120\n"
    "ds_read_b64_tr_b16 v[158:159], v9 offset:45888\n"
    "v_mfma_f32_16x16x32_bf16 v[52:55], v[132:135], v[108:111], v[52:55]\n"
    "ds_read_b64_tr_b16 v[160:161], v10 offset:45152\n"
    "ds_read_b64_tr_b16 v[162:163], v10 offset:45920\n"
    "v_mfma_f32_16x16x32_bf16 v[56:59], v[132:135], v[112:115], v[56:59]\n"
    "ds_read_b64_tr_b16 v[164:165], v9 offset:45184\n"
    "ds_read_b64_tr_b16 v[166:167], v9 offset:45952\n"
    "v_mfma_f32_16x16x32_bf16 v[60:63], v[136:139], v[108:111], v[60:63]\n"
    "ds_read_b64_tr_b16 v[168:169], v10 offset:45216\n"
    "ds_read_b64_tr_b16 v[170:171], v10 offset:45984\n"
    "v_mfma_f32_16x16x32_bf16 v[64:67], v[136:139], v[112:115], v[64:67]\n"
    "v_mfma_f32_16x16x32_bf16 v[68:71], v[140:143], v[108:111], v[68:71]\n"
    "v_mfma_f32_16x16x32_bf16 v[72:75], v[140:143], v[112:115], v[72:75]\n"
    "v_mfma_f32_16x16x32_bf16 v[76:79], v[144:147], v[108:111], v[76:79]\n"
    "v_mfma_f32_16x16x32_bf16 v[80:83], v[144:147], v[112:115], v[80:83]\n"
    "s_waitcnt lgkmcnt(0)\n"
    "s_barrier\n"
    "v_mfma_f32_16x16x32_bf16 v[36:39], v[148:151], v[116:119], v[36:39]\n"
    "ds_read_b128 v[108:111], v7 offset:0\n"
    "ds_read_b64_tr_b16 v[124:125], v11 offset:0\n"
    "v_mfma_f32_16x16x32_bf16 v[40:43], v[148:151], v[120:123], v[40:43]\n"
    "ds_read_b64_tr_b16 v[126:127], v11 offset:768\n"
    "ds_read_b128 v[112:115], v7 offset:512\n"
    "v_mfma_f32_16x16x32_bf16 v[44:47], v[152:155], v[116:119], v[44:47]\n"
    "ds_read_b64_tr_b16 v[128:129], v12 offset:32\n"
    "ds_read_b64_tr_b16 v[130:131], v12 offset:800\n"
    "v_mfma_f32_16x16x32_bf16 v[48:51], v[152:155], v[120:123], v[48:51]\n"
    "ds_read_b64_tr_b16 v[132:133], v11 offset:64\n"
    "ds_read_b64_tr_b16 v[134:135], v11 offset:832\n"
    "v_mfma_f32_16x16x32_bf16 v[52:55], v[156:159], v[116:119], v[52:55]\n"
    "ds_read_b64_tr_b16 v[136:137], v12 offset:96\n"
    "ds_read_b64_tr_b16 v[138:139], v12 offset:864\n"
    "v_mfma_f32_16x16x32_bf16 v[56:59], v[156:159], v[120:123], v[56:59]\n"
    "ds_read_b64_tr_b16 v[140:141], v11 offset:128\n"
    "ds_read_b64_tr_b16 v[142:143], v11 offset:896\n"
    "v_mfma_f32_16x16x32_bf16 v[60:63], v[160:163], v[116:119], v[60:63]\n"
    "ds_read_b64_tr_b16 v[144:145], v12 offset:160\n"
    "ds_read_b64_tr_b16 v[146:147], v12 offset:928\n"
    "v_mfma_f32_16x16x32_bf16 v[64:67], v[160:163], v[120:123], v[64:67]\n"
    "v_mfma_f32_16x16x32_bf16 v[68:71], v[164:167], v[116:119], v[68:71]\n"
    "v_mfma_f32_16x16x32_bf16 v[72:75], v[164:167], v[120:123], v[72:75]\n"
    "v_mfma_f32_16x16x32_bf16 v[76:79], v[168:171], v[116:119], v[76:79]\n"
    "v_mfma_f32_16x16x32_bf16 v[80:83], v[168:171], v[120:123], v[80:83]\n"
    "s_waitcnt lgkmcnt(0)\n"
    "s_waitcnt vmcnt(0)\n"
    "s_setprio 1\n"
    "v_mfma_f32_16x16x32_bf16 v[36:39], v[124:127], v[108:111], v[36:39]\n"
    "buffer_store_dword v251, v250, s[24:27], s77 offen sc1\n"
    "ds_read_b128 v[116:119], v8 offset:0\n"
    "ds_read_b64_tr_b16 v[148:149], v11 offset:6144\n"
    "v_mfma_f32_16x16x32_bf16 v[40:43], v[124:127], v[112:115], v[40:43]\n"
    "ds_read_b64_tr_b16 v[150:151], v11 offset:6912\n"
    "ds_read_b128 v[120:123], v8 offset:512\n"
    "v_mfma_f32_16x16x32_bf16 v[44:47], v[128:131], v[108:111], v[44:47]\n"
    "ds_read_b64_tr_b16 v[152:153], v12 offset:6176\n"
    "ds_read_b64_tr_b16 v[154:155], v12 offset:6944\n"
    "v_mfma_f32_16x16x32_bf16 v[48:51], v[128:131], v[112:115], v[48:51]\n"
    "ds_read_b64_tr_b16 v[156:157], v11 offset:6208\n"
    "ds_read_b64_tr_b16 v[158:159], v11 offset:6976\n"
    "v_mfma_f32_16x16x32_bf16 v[52:55], v[132:135], v[108:111], v[52:55]\n"
    "ds_read_b64_tr_b16 v[160:161], v12 offset:6240\n"
    "ds_read_b64_tr_b16 v[162:163], v12 offset:7008\n"
    "v_mfma_f32_16x16x32_bf16 v[56:59], v[132:135], v[112:115], v[56:59]\n"
    "ds_read_b64_tr_b16 v[164:165], v11 offset:6272\n"
    "ds_read_b64_tr_b16 v[166:167], v11 offset:7040\n"
    "v_mfma_f32_16x16x32_bf16 v[60:63], v[136:139], v[108:111], v[60:63]\n"
    "ds_read_b64_tr_b16 v[168:169], v12 offset:6304\n"
    "ds_read_b64_tr_b16 v[170:171], v12 offset:7072\n"
    "v_mfma_f32_16x16x32_bf16 v[64:67], v[136:139], v[112:115], v[64:67]\n"
    "v_mfma_f32_16x16x32_bf16 v[68:71], v[140:143], v[108:111], v[68:71]\n"
    "v_mfma_f32_16x16x32_bf16 v[72:75], v[140:143], v[112:115], v[72:75]\n"
    "v_mfma_f32_16x16x32_bf16 v[76:79], v[144:147], v[108:111], v[76:79]\n"
    "v_mfma_f32_16x16x32_bf16 v[80:83], v[144:147], v[112:115], v[80:83]\n"
    "s_setprio 0\n"
    "s_waitcnt lgkmcnt(0)\n"
    "s_barrier\n"
    "s_setprio 1\n"
    "v_mfma_f32_16x16x32_bf16 v[36:39], v[148:151], v[116:119], v[36:39]\n"
    "buffer_load_dwordx4 v[172:175], v194, s[24:27], s17 offen sc1\n"
    "ds_read_b128 v[108:111], v5 offset:0\n"
    "ds_read_b64_tr_b16 v[124:125], v9 offset:0\n"
    "v_mfma_f32_16x16x32_bf16 v[40:43], v[148:151], v[120:123], v[40:43]\n"
    "ds_read_b64_tr_b16 v[126:127], v9 offset:768\n"
    "ds_read_b128 v[112:115], v5 offset:512\n"
    "v_mfma_f32_16x16x32_bf16 v[44:47], v[152:155], v[116:119], v[44:47]\n"
    "ds_read_b64_tr_b16 v[128:129], v10 offset:32\n"
    "ds_read_b64_tr_b16 v[130:131], v10 offset:800\n"
    "buffer_load_dwordx4 v[176:179], v194, s[24:27], s18 offen sc1\n"
    "v_mfma_f32_16x16x32_bf16 v[48:51], v[152:155], v[120:123], v[48:51]\n"
    "ds_read_b64_tr_b16 v[132:133], v9 offset:64\n"
    "ds_read_b64_tr_b16 v[134:135], v9 offset:832\n"
    "v_mfma_f32_16x16x32_bf16 v[52:55], v[156:159], v[116:119], v[52:55]\n"
    "ds_read_b64_tr_b16 v[136:137], v10 offset:96\n"
    "ds_read_b64_tr_b16 v[138:139], v10 offset:864\n"
    "v_mfma_f32_16x16x32_bf16 v[56:59], v[156:159], v[120:123], v[56:59]\n"
    "ds_read_b64_tr_b16 v[140:141], v9 offset:128\n"
    "ds_read_b64_tr_b16 v[142:143], v9 offset:896\n"
    "buffer_load_dwordx4 v[180:183], v195, s[24:27], s35 offen sc1\n"
    "v_mfma_f32_16x16x32_bf16 v[60:63], v[160:163], v[116:119], v[60:63]\n"
    "ds_read_b64_tr_b16 v[144:145], v10 offset:160\n"
    "ds_read_b64_tr_b16 v[146:147], v10 offset:928\n"
    "v_mfma_f32_16x16x32_bf16 v[64:67], v[160:163], v[120:123], v[64:67]\n"
    "v_mfma_f32_16x16x32_bf16 v[68:71], v[164:167], v[116:119], v[68:71]\n"
    "v_mfma_f32_16x16x32_bf16 v[72:75], v[164:167], v[120:123], v[72:75]\n"
    "v_mfma_f32_16x16x32_bf16 v[76:79], v[168:171], v[116:119], v[76:79]\n"
    "v_mfma_f32_16x16x32_bf16 v[80:83], v[168:171], v[120:123], v[80:83]\n"
    "s_setprio 0\n"
    "s_waitcnt lgkmcnt(0)\n"
    "v_mfma_f32_16x16x32_bf16 v[36:39], v[124:127], v[108:111], v[36:39]\n"
    "ds_read_b128 v[116:119], v6 offset:0\n"
    "ds_read_b64_tr_b16 v[148:149], v9 offset:6144\n"
    "v_mfma_f32_16x16x32_bf16 v[40:43], v[124:127], v[112:115], v[40:43]\n"
    "ds_read_b64_tr_b16 v[150:151], v9 offset:6912\n"
    "ds_read_b128 v[120:123], v6 offset:512\n"
    "v_mfma_f32_16x16x32_bf16 v[44:47], v[128:131], v[108:111], v[44:47]\n"
    "ds_read_b64_tr_b16 v[152:153], v10 offset:6176\n"
    "ds_read_b64_tr_b16 v[154:155], v10 offset:6944\n"
    "v_mfma_f32_16x16x32_bf16 v[48:51], v[128:131], v[112:115], v[48:51]\n"
    "ds_read_b64_tr_b16 v[156:157], v9 offset:6208\n"
    "ds_read_b64_tr_b16 v[158:159], v9 offset:6976\n"
    "v_mfma_f32_16x16x32_bf16 v[52:55], v[132:135], v[108:111], v[52:55]\n"
    "ds_read_b64_tr_b16 v[160:161], v10 offset:6240\n"
    "ds_read_b64_tr_b16 v[162:163], v10 offset:7008\n"
    "v_mfma_f32_16x16x32_bf16 v[56:59], v[132:135], v[112:115], v[56:59]\n"
    "ds_read_b64_tr_b16 v[164:165], v9 offset:6272\n"
    "ds_read_b64_tr_b16 v[166:167], v9 offset:7040\n"
    "v_mfma_f32_16x16x32_bf16 v[60:63], v[136:139], v[108:111], v[60:63]\n"
    "ds_read_b64_tr_b16 v[168:169], v10 offset:6304\n"
    "ds_read_b64_tr_b16 v[170:171], v10 offset:7072\n"
    "v_mfma_f32_16x16x32_bf16 v[64:67], v[136:139], v[112:115], v[64:67]\n"
    "v_mfma_f32_16x16x32_bf16 v[68:71], v[140:143], v[108:111], v[68:71]\n"
    "v_mfma_f32_16x16x32_bf16 v[72:75], v[140:143], v[112:115], v[72:75]\n"
    "v_mfma_f32_16x16x32_bf16 v[76:79], v[144:147], v[108:111], v[76:79]\n"
    "v_mfma_f32_16x16x32_bf16 v[80:83], v[144:147], v[112:115], v[80:83]\n"
    "s_waitcnt lgkmcnt(0)\n"
    "s_barrier\n"
    "v_mfma_f32_16x16x32_bf16 v[36:39], v[148:151], v[116:119], v[36:39]\n"
    "ds_read_b128 v[108:111], v5 offset:38912\n"
    "ds_read_b64_tr_b16 v[124:125], v9 offset:38912\n"
    "v_mfma_f32_16x16x32_bf16 v[40:43], v[148:151], v[120:123], v[40:43]\n"
    "ds_read_b64_tr_b16 v[126:127], v9 offset:39680\n"
    "ds_read_b128 v[112:115], v5 offset:39424\n"
    "v_mfma_f32_16x16x32_bf16 v[44:47], v[152:155], v[116:119], v[44:47]\n"
    "ds_read_b64_tr_b16 v[128:129], v10 offset:38944\n"
    "ds_read_b64_tr_b16 v[130:131], v10 offset:39712\n"
    "v_mfma_f32_16x16x32_bf16 v[48:51], v[152:155], v[120:123], v[48:51]\n"
    "ds_read_b64_tr_b16 v[132:133], v9 offset:38976\n"
    "ds_read_b64_tr_b16 v[134:135], v9 offset:39744\n"
    "v_mfma_f32_16x16x32_bf16 v[52:55], v[156:159], v[116:119], v[52:55]\n"
    "ds_read_b64_tr_b16 v[136:137], v10 offset:39008\n"
    "ds_read_b64_tr_b16 v[138:139], v10 offset:39776\n"
    "v_mfma_f32_16x16x32_bf16 v[56:59], v[156:159], v[120:123], v[56:59]\n"
    "ds_read_b64_tr_b16 v[140:141], v9 offset:39040\n"
    "ds_read_b64_tr_b16 v[142:143], v9 offset:39808\n"
    "v_mfma_f32_16x16x32_bf16 v[60:63], v[160:163], v[116:119], v[60:63]\n"
    "ds_read_b64_tr_b16 v[144:145], v10 offset:39072\n"
    "ds_read_b64_tr_b16 v[146:147], v10 offset:39840\n"
    "v_mfma_f32_16x16x32_bf16 v[64:67], v[160:163], v[120:123], v[64:67]\n"
    "v_mfma_f32_16x16x32_bf16 v[68:71], v[164:167], v[116:119], v[68:71]\n"
    "v_mfma_f32_16x16x32_bf16 v[72:75], v[164:167], v[120:123], v[72:75]\n"
    "v_mfma_f32_16x16x32_bf16 v[76:79], v[168:171], v[116:119], v[76:79]\n"
    "v_mfma_f32_16x16x32_bf16 v[80:83], v[168:171], v[120:123], v[80:83]\n"
    "s_waitcnt lgkmcnt(0)\n"
    "v_mfma_f32_16x16x32_bf16 v[36:39], v[124:127], v[108:111], v[36:39]\n"
    "s_waitcnt vmcnt(0)\n"
    "v_xor_b32 v29, s73, v181\n"
    "ds_read_b128 v[116:119], v6 offset:38912\n"
    "ds_read_b64_tr_b16 v[148:149], v9 offset:45056\n"
    "v_xor_b32 v28, s73, v183\n"
    "v_mfma_f32_16x16x32_bf16 v[40:43], v[124:127], v[112:115], v[40:43]\n"
    "ds_read_b64_tr_b16 v[150:151], v9 offset:45824\n"
    "v_or_b32 v28, v28, v29\n"
    "ds_read_b128 v[120:123], v6 offset:39424\n"
    "v_mfma_f32_16x16x32_bf16 v[44:47], v[128:131], v[108:111], v[44:47]\n"
    "ds_read_b64_tr_b16 v[152:153], v10 offset:45088\n"
    "v_cndmask_b32 v28, 0, v28, s[36:37]\n"
    "ds_read_b64_tr_b16 v[154:155], v10 offset:45856\n"
    "v_mfma_f32_16x16x32_bf16 v[48:51], v[128:131], v[112:115], v[48:51]\n"
    "v_xor_b32 v29, s73, v173\n"
    "ds_read_b64_tr_b16 v[156:157], v9 offset:45120\n"
    "ds_read_b64_tr_b16 v[158:159], v9 offset:45888\n"
    "v_xor_b32 v30, s73, v175\n"
    "v_mfma_f32_16x16x32_bf16 v[52:55], v[132:135], v[108:111], v[52:55]\n"
    "ds_read_b64_tr_b16 v[160:161], v10 offset:45152\n"
    "ds_read_b64_tr_b16 v[162:163], v10 offset:45920\n"
    "v_or3_b32 v28, v28, v29, v30\n"
    "v_mfma_f32_16x16x32_bf16 v[56:59], v[132:135], v[112:115], v[56:59]\n"
    "ds_read_b64_tr_b16 v[164:165], v9 offset:45184\n"
    "v_xor_b32 v29, s73, v177\n"
    "ds_read_b64_tr_b16 v[166:167], v9 offset:45952\n"
    "v_mfma_f32_16x16x32_bf16 v[60:63], v[136:139], v[108:111], v[60:63]\n"
    "v_xor_b32 v30, s73, v179\n"
    "ds_read_b64_tr_b16 v[168:169], v10 offset:45216\n"
    "ds_read_b64_tr_b16 v[170:171], v10 offset:45984\n"
    "v_mfma_f32_16x16x32_bf16 v[64:67], v[136:139], v[112:115], v[64:67]\n"
    "v_or3_b32 v28, v28, v29, v30\n"
    "v_mfma_f32_16x16x32_bf16 v[68:71], v[140:143], v[108:111], v[68:71]\n"
    "v_mfma_f32_16x16x32_bf16 v[72:75], v[140:143], v[112:115], v[72:75]\n"
    "v_cmp_ne_u32 vcc, 0, v28\n"
    "v_mfma_f32_16x16x32_bf16 v[76:79], v[144:147], v[108:111], v[76:79]\n"
    "v_mfma_f32_16x16x32_bf16 v[80:83], v[144:147], v[112:115], v[80:83]\n"
    "s_nop 4\n"
    "s_cbranch_vccz Lk_skip_64\n"
    "s_mov_b32 s0, 131072\n"
    "s_nop 4\n"
    "Lk_poll_65:\n"
    "buffer_load_dwordx4 v[172:175], v194, s[24:27], s17 offen sc1\n"
    "buffer_load_dwordx4 v[176:179], v194, s[24:27], s18 offen sc1\n"
    "buffer_load_dwordx4 v[180:183], v195, s[24:27], s35 offen sc1\n"
    "Lk_chk_67:\n"
    "s_waitcnt vmcnt(0)\n"
    "v_xor_b32 v29, s73, v181\n"
    "v_xor_b32 v28, s73, v183\n"
    "v_or_b32 v28, v28, v29\n"
    "v_cndmask_b32 v28, 0, v28, s[36:37]\n"
    "v_xor_b32 v29, s73, v173\n"
    "v_xor_b32 v30, s73, v175\n"
    "v_or3_b32 v28, v28, v29, v30\n"
    "v_xor_b32 v29, s73, v177\n"
    "v_xor_b32 v30, s73, v179\n"
    "v_or3_b32 v28, v28, v29, v30\n"
    "v_cmp_ne_u32 vcc, 0, v28\n"
    "s_nop 4\n"
    "s_cbranch_vccz Lk_done_66\n"
    "s_sleep 4\n"
    "s_sub_u32 s0, s0, 1\n"
    "s_cbranch_scc0 Lk_poll_65\n"
    "Lk_done_66:\n"
    "Lk_skip_64:\n"
    "s_waitcnt lgkmcnt(0)\n"
    "s_barrier\n"
    "s_setprio 1\n"
    "v_mfma_f32_16x16x32_bf16 v[36:39], v[148:151], v[116:119], v[36:39]\n"
    "v_add_f32 v172, v172, v176\n"
    "buffer_load_dword v199, v198, s[24:27], 0 offen sc1\n"
    "ds_read_b128 v[108:111], v7 offset:0\n"
    "v_add_f32 v174, v174, v178\n"
    "ds_read_b64_tr_b16 v[124:125], v11 offset:0\n"
    "v_add_f32 v172, v172, v180\n"
    "v_mfma_f32_16x16x32_bf16 v[40:43], v[148:151], v[120:123], v[40:43]\n"
    "ds_read_b64_tr_b16 v[126:127], v11 offset:768\n"
    "v_add_f32 v173, v174, v182\n"
    "ds_read_b128 v[112:115], v7 offset:512\n"
    "v_mov_b32 v174, v172\n"
    "v_mfma_f32_16x16x32_bf16 v[44:47], v[152:155], v[116:119], v[44:47]\n"
    "ds_read_b64_tr_b16 v[128:129], v12 offset:32\n"
    "v_mov_b32 v175, v173\n"
    "ds_read_b64_tr_b16 v[130:131], v12 offset:800\n"
    "v_permlane16_swap_b32 v172, v174\n"
    "v_mfma_f32_16x16x32_bf16 v[48:51], v[152:155], v[120:123], v[48:51]\n"
    "ds_read_b64_tr_b16 v[132:133], v11 offset:64\n"
    "v_permlane16_swap_b32 v173, v175\n"
    "ds_read_b64_tr_b16 v[134:135], v11 offset:832\n"
    "v_add_f32 v172, v172, v174\n"
    "v_mfma_f32_16x16x32_bf16 v[52:55], v[156:159], v[116:119], v[52:55]\n"
    "ds_read_b64_tr_b16 v[136:137], v12 offset:96\n"
    "v_add_f32 v173, v173, v175\n"
    "ds_read_b64_tr_b16 v[138:139], v12 offset:864\n"
    "v_mov_b32 v174, v172\n"
    "v_mfma_f32_16x16x32_bf16 v[56:59], v[156:159], v[120:123], v[56:59]\n"
    "v_mov_b32 v175, v173\n"
    "ds_read_b64_tr_b16 v[140:141], v11 offset:128\n"
    "ds_read_b64_tr_b16 v[142:143], v11 offset:896\n"
    "v_permlane32_swap_b32 v172, v174\n"
    "v_mfma_f32_16x16x32_bf16 v[60:63], v[160:163], v[116:119], v[60:63]\n"
    "v_permlane32_swap_b32 v173, v175\n"
    "ds_read_b64_tr_b16 v[144:145], v12 offset:160\n"
    "ds_read_b64_tr_b16 v[146:147], v12 offset:928\n"
    "v_add_f32 v172, v172, v174\n"
    "v_mfma_f32_16x16x32_bf16 v[64:67], v[160:163], v[120:123], v[64:67]\n"
    "v_add_f32 v173, v173, v175\n"
    "v_mfma_f32_16x16x32_bf16 v[68:71], v[164:167], v[116:119], v[68:71]\n"
    "v_mfma_f32_16x16x32_bf16 v[72:75], v[164:167], v[120:123], v[72:75]\n"
    "v_and_b32 v174, 0xffff, v13\n"
    "v_mfma_f32_16x16x32_bf16 v[76:79], v[168:171], v[116:119], v[76:79]\n"
    "v_lshrrev_b32 v175, 16, v13\n"
    "v_mfma_f32_16x16x32_bf16 v[80:83], v[168:171], v[120:123], v[80:83]\n"
    "s_setprio 0\n"
    "s_waitcnt lgkmcnt(0)\n"
    "v_mfma_f32_16x16x32_bf16 v[36:39], v[124:127], v[108:111], v[36:39]\n"
    "v_and_b32 v177, s1, v172\n"
    "ds_read_b128 v[116:119], v8 offset:0\n"
    "v_and_b32 v181, s1, v173\n"
    "ds_read_b64_tr_b16 v[148:149], v11 offset:6144\n"
    "v_sub_f32 v178, v172, v177\n"
    "v_mfma_f32_16x16x32_bf16 v[40:43], v[124:127], v[112:115], v[40:43]\n"
    "ds_read_b64_tr_b16 v[150:151], v11 offset:6912\n"
    "v_sub_f32 v182, v173, v181\n"
    "ds_read_b128 v[120:123], v8 offset:512\n"
    "v_and_b32 v29, s1, v178\n"
    "v_mfma_f32_16x16x32_bf16 v[44:47], v[128:131], v[108:111], v[44:47]\n"
    "ds_read_b64_tr_b16 v[152:153], v12 offset:6176\n"
    "v_and_b32 v30, s1, v182\n"
    "ds_read_b64_tr_b16 v[154:155], v12 offset:6944\n"
    "v_cvt_pk_bf16_f32 v176, v177, v29\n"
    "v_mfma_f32_16x16x32_bf16 v[48:51], v[128:131], v[112:115], v[48:51]\n"
    "ds_read_b64_tr_b16 v[156:157], v11 offset:6208\n"
    "v_cvt_pk_bf16_f32 v180, v181, v30\n"
    "ds_read_b64_tr_b16 v[158:159], v11 offset:6976\n"
    "v_cvt_pk_bf16_f32 v179, v29, v177\n"
    "v_mfma_f32_16x16x32_bf16 v[52:55], v[132:135], v[108:111], v[52:55]\n"
    "ds_read_b64_tr_b16 v[160:161], v12 offset:6240\n"
    "v_cvt_pk_bf16_f32 v183, v30, v181\n"
    "ds_read_b64_tr_b16 v[162:163], v12 offset:7008\n"
    "v_sub_f32 v178, v178, v29\n"
    "v_mfma_f32_16x16x32_bf16 v[56:59], v[132:135], v[112:115], v[56:59]\n"
    "v_sub_f32 v182, v182, v30\n"
    "ds_read_b64_tr_b16 v[164:165], v11 offset:6272\n"
    "ds_read_b64_tr_b16 v[166:167], v11 offset:7040\n"
    "v_alignbit_b32 v177, v174, v172, 16\n"
    "v_mfma_f32_16x16x32_bf16 v[60:63], v[136:139], v[108:111], v[60:63]\n"
    "v_alignbit_b32 v181, v175, v173, 16\n"
    "ds_read_b64_tr_b16 v[168:169], v12 offset:6304\n"
    "ds_read_b64_tr_b16 v[170:171], v12 offset:7072\n"
    "v_and_or_b32 v178, v178, s1, v174\n"
    "v_mfma_f32_16x16x32_bf16 v[64:67], v[136:139], v[112:115], v[64:67]\n"
    "v_and_or_b32 v182, v182, s1, v175\n"
    "v_mfma_f32_16x16x32_bf16 v[68:71], v[140:143], v[108:111], v[68:71]\n"
    "v_mfma_f32_16x16x32_bf16 v[72:75], v[140:143], v[112:115], v[72:75]\n"
    "ds_write_b128 v196, v[176:179] offset:0\n"
    "v_mfma_f32_16x16x32_bf16 v[76:79], v[144:147], v[108:111], v[76:79]\n"
    "ds_write_b128 v196, v[180:183] offset:16\n"
    "v_mfma_f32_16x16x32_bf16 v[80:83], v[144:147], v[112:115], v[80:83]\n"
    "s_waitcnt vmcnt(0)\n"
    "v_cmp_ne_u32 vcc, s73, v199\n"
    "s_nop 4\n"
    "s_cbranch_vccz Lk_skip_68\n"
    "s_mov_b32 s0, 131072\n"
    "s_nop 4\n"
    "Lk_poll_69:\n"
    "buffer_load_dword v199, v198, s[24:27], 0 offen sc1\n"
    "Lk_chk_71:\n"
    "s_waitcnt vmcnt(0)\n"
    "v_cmp_ne_u32 vcc, s73, v199\n"
    "s_nop 4\n"
    "s_cbranch_vccz Lk_done_70\n"
    "s_sleep 4\n"
    "s_sub_u32 s0, s0, 1\n"
    "s_cbranch_scc0 Lk_poll_69\n"
    "Lk_done_70:\n"
    "Lk_skip_68:\n"
    "s_waitcnt lgkmcnt(0)\n"
    "s_barrier\n"
    "ds_read_b128 v[24:27], v16 offset:0\n"
    "ds_read_b32 v21, v14 offset:1536\n"
    "ds_read_b32 v32, v14 offset:1024\n"
    "ds_read_b128 v[28:31], v16 offset:256\n"
    "ds_read_b32 v22, v14 offset:1600\n"
    "ds_read_b32 v33, v14 offset:1088\n"
    "ds_read_b128 v[84:87], v15 offset:0\n"
    "ds_read_b128 v[88:91], v15 offset:256\n"
    "ds_read_b128 v[92:95], v15 offset:512\n"
    "ds_read_b128 v[96:99], v15 offset:768\n"
    "ds_read_b128 v[100:103], v15 offset:1024\n"
    "ds_read_b128 v[104:107], v15 offset:1280\n"
    "v_mfma_f32_16x16x32_bf16 v[36:39], v[148:151], v[116:119], v[36:39]\n"
    "ds_read_b128 v[108:111], v5 offset:0\n"
    "ds_read_b64_tr_b16 v[124:125], v9 offset:0\n"
    "v_mfma_f32_16x16x32_bf16 v[40:43], v[148:151], v[120:123], v[40:43]\n"
    "ds_read_b64_tr_b16 v[126:127], v9 offset:768\n"
    "ds_read_b128 v[112:115], v5 offset:512\n"
    "v_mfma_f32_16x16x32_bf16 v[44:47], v[152:155], v[116:119], v[44:47]\n"
    "ds_read_b64_tr_b16 v[128:129], v10 offset:32\n"
    "ds_read_b64_tr_b16 v[130:131], v10 offset:800\n"
    "v_mfma_f32_16x16x32_bf16 v[48:51], v[152:155], v[120:123], v[48:51]\n"
    "ds_read_b64_tr_b16 v[132:133], v9 offset:64\n"
    "ds_read_b64_tr_b16 v[134:135], v9 offset:832\n"
    "v_mfma_f32_16x16x32_bf16 v[52:55], v[156:159], v[116:119], v[52:55]\n"
    "ds_read_b64_tr_b16 v[136:137], v10 offset:96\n"
    "ds_read_b64_tr_b16 v[138:139], v10 offset:864\n"
    "v_mfma_f32_16x16x32_bf16 v[56:59], v[156:159], v[120:123], v[56:59]\n"
    "ds_read_b64_tr_b16 v[140:141], v9 offset:128\n"
    "ds_read_b64_tr_b16 v[142:143], v9 offset:896\n"
    "v_mfma_f32_16x16x32_bf16 v[60:63], v[160:163], v[116:119], v[60:63]\n"
    "ds_read_b64_tr_b16 v[144:145], v10 offset:160\n"
    "ds_read_b64_tr_b16 v[146:147], v10 offset:928\n"
    "v_mfma_f32_16x16x32_bf16 v[64:67], v[160:163], v[120:123], v[64:67]\n"
    "v_mfma_f32_16x16x32_bf16 v[68:71], v[164:167], v[116:119], v[68:71]\n"
    "v_mfma_f32_16x16x32_bf16 v[72:75], v[164:167], v[120:123], v[72:75]\n"
    "v_mfma_f32_16x16x32_bf16 v[76:79], v[168:171], v[116:119], v[76:79]\n"
    "v_mfma_f32_16x16x32_bf16 v[80:83], v[168:171], v[120:123], v[80:83]\n"
    "s_waitcnt lgkmcnt(0)\n"
    "v_mfma_f32_16x16x32_bf16 v[36:39], v[124:127], v[108:111], v[36:39]\n"
    "ds_read_b128 v[116:119], v6 offset:0\n"
    "ds_read_b64_tr_b16 v[148:149], v9 offset:6144\n"
    "v_mfma_f32_16x16x32_bf16 v[40:43], v[124:127], v[112:115], v[40:43]\n"
    "ds_read_b64_tr_b16 v[150:151], v9 offset:6912\n"
    "ds_read_b128 v[120:123], v6 offset:512\n"
    "v_mfma_f32_16x16x32_bf16 v[44:47], v[128:131], v[108:111], v[44:47]\n"
    "ds_read_b64_tr_b16 v[152:153], v10 offset:6176\n"
    "ds_read_b64_tr_b16 v[154:155], v10 offset:6944\n"
    "v_mfma_f32_16x16x32_bf16 v[48:51], v[128:131], v[112:115], v[48:51]\n"
    "ds_read_b64_tr_b16 v[156:157], v9 offset:6208\n"
    "ds_read_b64_tr_b16 v[158:159], v9 offset:6976\n"
    "v_mfma_f32_16x16x32_bf16 v[52:55], v[132:135], v[108:111], v[52:55]\n"
    "ds_read_b64_tr_b16 v[160:161], v10 offset:6240\n"
    "ds_read_b64_tr_b16 v[162:163], v10 offset:7008\n"
    "v_mfma_f32_16x16x32_bf16 v[56:59], v[132:135], v[112:115], v[56:59]\n"
    "ds_read_b64_tr_b16 v[164:165], v9 offset:6272\n"
    "ds_read_b64_tr_b16 v[166:167], v9 offset:7040\n"
    "v_mfma_f32_16x16x32_bf16 v[60:63], v[136:139], v[108:111], v[60:63]\n"
    "ds_read_b64_tr_b16 v[168:169], v10 offset:6304\n"
    "ds_read_b64_tr_b16 v[170:171], v10 offset:7072\n"
    "v_mfma_f32_16x16x32_bf16 v[64:67], v[136:139], v[112:115], v[64:67]\n"
    "v_mfma_f32_16x16x32_bf16 v[68:71], v[140:143], v[108:111], v[68:71]\n"
    "v_mfma_f32_16x16x32_bf16 v[72:75], v[140:143], v[112:115], v[72:75]\n"
    "v_mfma_f32_16x16x32_bf16 v[76:79], v[144:147], v[108:111], v[76:79]\n"
    "v_mfma_f32_16x16x32_bf16 v[80:83], v[144:147], v[112:115], v[80:83]\n"
    "s_waitcnt lgkmcnt(0)\n"
    "s_barrier\n"
    "v_mfma_f32_16x16x32_bf16 v[36:39], v[148:151], v[116:119], v[36:39]\n"
    "v_mul_f32 v8, 0xc0135761, v32\n"
    "v_mul_f32 v7, v32, v32\n"
    "v_mfma_f32_16x16x32_bf16 v[40:43], v[148:151], v[120:123], v[40:43]\n"
    "v_mul_f32 v6, 0x3d372713, v8\n"
    "v_mfma_f32_16x16x32_bf16 v[44:47], v[152:155], v[116:119], v[44:47]\n"
    "v_mov_b32 v9, v8\n"
    "v_mfma_f32_16x16x32_bf16 v[48:51], v[152:155], v[120:123], v[48:51]\n"
    "v_mul_f32 v6, v6, v7\n"
    "v_mov_b32 v10, v21\n"
    "v_mfma_f32_16x16x32_bf16 v[52:55], v[156:159], v[116:119], v[52:55]\n"
    "v_mov_b32 v7, v6\n"
    "v_mfma_f32_16x16x32_bf16 v[56:59], v[156:159], v[120:123], v[56:59]\n"
    "v_mov_b32 v11, v21\n"
    "v_mfma_f32_16x16x32_bf16 v[60:63], v[160:163], v[116:119], v[60:63]\n"
    "v_mul_f32 v14, 0xc0135761, v33\n"
    "v_mul_f32 v13, v33, v33\n"
    "v_mfma_f32_16x16x32_bf16 v[64:67], v[160:163], v[120:123], v[64:67]\n"
    "v_mul_f32 v12, 0x3d372713, v14\n"
    "v_mfma_f32_16x16x32_bf16 v[68:71], v[164:167], v[116:119], v[68:71]\n"
    "v_mov_b32 v15, v14\n"
    "v_mfma_f32_16x16x32_bf16 v[72:75], v[164:167], v[120:123], v[72:75]\n"
    "v_mul_f32 v12, v12, v13\n"
    "v_mov_b32 v16, v22\n"
    "v_mfma_f32_16x16x32_bf16 v[76:79], v[168:171], v[116:119], v[76:79]\n"
    "v_mov_b32 v13, v12\n"
    "v_mfma_f32_16x16x32_bf16 v[80:83], v[168:171], v[120:123], v[80:83]\n"
    "v_mov_b32 v17, v22\n"
    "v_mfma_f32_16x16x32_bf16 v[36:39], v[84:87], v[24:27], v[36:39]\n"
    "s_mul_i32 s95, s79, 24\n"
    "s_mul_i32 s92, s68, 4\n"
    "s_add_u32 s95, s95, s92\n"
    "s_mul_i32 s92, s72, 88\n"
    "v_mfma_f32_16x16x32_bf16 v[40:43], v[84:87], v[28:31], v[40:43]\n"
    "s_add_u32 s92, s92, s95\n"
    "s_mul_i32 s92, s92, 5120\n"
    "s_add_u32 s28, s92, 262144\n"
    "s_add_u32 s29, s92, 267264\n"
    "v_mfma_f32_16x16x32_bf16 v[44:47], v[88:91], v[24:27], v[44:47]\n"
    "s_add_u32 s30, s92, 272384\n"
    "s_add_u32 s31, s92, 277504\n"
    "s_mul_i32 s95, s79, 20\n"
    "v_mfma_f32_16x16x32_bf16 v[48:51], v[88:91], v[28:31], v[48:51]\n"
    "s_mul_i32 s92, s68, 2\n"
    "s_add_u32 s95, s95, s92\n"
    "s_mul_i32 s92, s72, 88\n"
    "s_add_u32 s92, s92, s95\n"
    "v_mfma_f32_16x16x32_bf16 v[52:55], v[92:95], v[24:27], v[52:55]\n"
    "s_mul_i32 s92, s92, 5120\n"
    "s_add_u32 s32, s92, 507904\n"
    "s_add_u32 s33, s92, 513024\n"
    "v_mfma_f32_16x16x32_bf16 v[56:59], v[92:95], v[28:31], v[56:59]\n"
    "s_lshr_b32 s92, s68, 1\n"
    "s_lshl_b32 s93, s69, 1\n"
    "s_add_u32 s94, s92, s93\n"
    "s_mul_i32 s94, s94, 360448\n"
    "v_mfma_f32_16x16x32_bf16 v[60:63], v[96:99], v[24:27], v[60:63]\n"
    "s_and_b32 s93, s68, 1\n"
    "s_lshl_b32 s93, s93, 11\n"
    "s_add_u32 s94, s94, s93\n"
    "s_add_u32 s94, s94, 4194304\n"
    "v_mfma_f32_16x16x32_bf16 v[64:67], v[96:99], v[28:31], v[64:67]\n"
    "s_mul_i32 s93, s70, 12288\n"
    "s_add_u32 s52, s94, s93\n"
    "s_add_u32 s53, s52, 4096\n"
    "v_mfma_f32_16x16x32_bf16 v[68:71], v[100:103], v[24:27], v[68:71]\n"
    "s_add_u32 s54, s52, 8192\n"
    "s_xor_b32 s92, s71, 1\n"
    "s_lshl_b32 s92, s92, 1\n"
    "s_lshr_b32 s93, s68, 1\n"
    "v_mfma_f32_16x16x32_bf16 v[72:75], v[100:103], v[28:31], v[72:75]\n"
    "s_add_u32 s92, s92, s93\n"
    "s_lshl_b32 s92, s92, 3\n"
    "s_and_b32 s93, s68, 1\n"
    "v_mfma_f32_16x16x32_bf16 v[76:79], v[104:107], v[24:27], v[76:79]\n"
    "s_add_u32 s92, s92, s93\n"
    "s_mul_i32 s92, s92, 5120\n"
    "s_mul_i32 s93, s72, 160\n"
    "s_add_u32 s19, s92, s93\n"
    "v_mfma_f32_16x16x32_bf16 v[80:83], v[104:107], v[28:31], v[80:83]\n"
    "s_mul_i32 s20, s72, 160\n"
    "s_mul_i32 s89, s68, 5120\n"
    "s_add_u32 s89, s89, 61440\n"
    "v_pk_mul_f32 v[116:117], v[36:37], v[36:37]\n"
    "buffer_load_dwordx4 v[152:155], v3, s[24:27], s28 offen offset:0 sc1\n"
    "v_add_u32 v29, 0, v0\n"
    "v_pk_mul_f32 v[118:119], v[38:39], v[38:39]\n"
    "v_mul_u32_u24 v30, 6554, v29\n"
    "v_pk_mul_f32 v[120:121], v[44:45], v[44:45]\n"
    "v_lshrrev_b32 v30, 16, v30\n"
    "v_pk_mul_f32 v[122:123], v[46:47], v[46:47]\n"
    "v_mul_u32_u24 v31, 10, v30\n"
    "v_pk_fma_f32 v[116:117], v[116:117], v[6:7], v[8:9]\n"
    "v_sub_u32 v29, v29, v31\n"
    "v_pk_fma_f32 v[118:119], v[118:119], v[6:7], v[8:9]\n"
    "v_pk_fma_f32 v[120:121], v[120:121], v[6:7], v[8:9]\n"
    "v_lshrrev_b32 v31, 2, v30\n"
    "v_pk_fma_f32 v[122:123], v[122:123], v[6:7], v[8:9]\n"
    "v_and_b32 v30, 3, v30\n"
    "v_pk_mul_f32 v[116:117], v[116:117], v[36:37]\n"
    "buffer_load_dwordx4 v[156:159], v3, s[24:27], s28 offen offset:1024 sc1\n"
    "v_lshl_add_u32 v30, v31, 3, v30\n"
    "v_pk_mul_f32 v[118:119], v[118:119], v[38:39]\n"
    "v_mul_u32_u24 v30, 1280, v30\n"
    "v_pk_mul_f32 v[120:121], v[120:121], v[44:45]\n"
    "v_lshl_add_u32 v252, v29, 4, v30\n"
    "v_pk_mul_f32 v[122:123], v[122:123], v[46:47]\n"
    "v_exp_f32 v116, v116\n"
    "v_add_u32 v29, 64, v0\n"
    "v_exp_f32 v117, v117\n"
    "v_mul_u32_u24 v30, 6554, v29\n"
    "v_exp_f32 v118, v118\n"
    "v_lshrrev_b32 v30, 16, v30\n"
    "v_exp_f32 v119, v119\n"
    "v_mul_u32_u24 v31, 10, v30\n"
    "v_exp_f32 v120, v120\n"
    "buffer_load_dwordx4 v[160:163], v3, s[24:27], s28 offen offset:2048 sc1\n"
    "v_sub_u32 v29, v29, v31\n"
    "v_exp_f32 v121, v121\n"
    "v_exp_f32 v122, v122\n"
    "v_lshrrev_b32 v31, 2, v30\n"
    "v_exp_f32 v123, v123\n"
    "v_and_b32 v30, 3, v30\n"
    "v_pk_fma_f32 v[116:117], v[116:117], v[10:11], v[10:11]\n"
    "v_lshl_add_u32 v30, v31, 3, v30\n"
    "v_pk_fma_f32 v[118:119], v[118:119], v[10:11], v[10:11]\n"
    "v_mul_u32_u24 v30, 1280, v30\n"
    "v_pk_fma_f32 v[120:121], v[120:121], v[10:11], v[10:11]\n"
    "v_pk_fma_f32 v[122:123], v[122:123], v[10:11], v[10:11]\n"
    "v_lshl_add_u32 v253, v29, 4, v30\n"
    "v_rcp_f32 v116, v116\n"
    "buffer_load_dwordx4 v[164:167], v3, s[24:27], s28 offen offset:3072 sc1\n"
    "v_add_u32 v29, 128, v0\n"
    "v_rcp_f32 v117, v117\n"
    "v_mul_u32_u24 v30, 6554, v29\n"
    "v_rcp_f32 v118, v118\n"
    "v_lshrrev_b32 v30, 16, v30\n"
    "v_rcp_f32 v119, v119\n"
    "v_mul_u32_u24 v31, 10, v30\n"
    "v_rcp_f32 v120, v120\n"
    "v_rcp_f32 v121, v121\n"
    "v_sub_u32 v29, v29, v31\n"
    "v_rcp_f32 v122, v122\n"
    "v_lshrrev_b32 v31, 2, v30\n"
    "v_rcp_f32 v123, v123\n"
    "v_and_b32 v30, 3, v30\n"
    "v_pk_mul_f32 v[36:37], v[36:37], v[116:117]\n"
    "s_add_u32 s95, s28, 4096\n"
    "buffer_load_dwordx4 v[168:171], v3, s[24:27], s95 offen sc1\n"
    "v_lshl_add_u32 v30, v31, 3, v30\n"
    "v_pk_mul_f32 v[38:39], v[38:39], v[118:119]\n"
    "v_mul_u32_u24 v30, 1280, v30\n"
    "v_pk_mul_f32 v[44:45], v[44:45], v[120:121]\n"
    "v_pk_mul_f32 v[46:47], v[46:47], v[122:123]\n"
    "v_lshl_add_u32 v254, v29, 4, v30\n"
    "v_cvt_pk_bf16_f32 v124, v36, v37\n"
    "v_mov_b32 v29, 0x40000000\n"
    "v_cvt_pk_bf16_f32 v125, v38, v39\n"
    "v_cmp_gt_u32 vcc, 32, v0\n"
    "s_nop 1\n"
    "v_cndmask_b32 v254, v29, v254, vcc\n"
    "v_cvt_pk_bf16_f32 v126, v44, v45\n"
    "v_cmp_gt_u32 vcc, 10, v0\n"
    "s_nop 1\n"
    "v_cndmask_b32 v255, v29, v3, vcc\n"
    "v_cvt_pk_bf16_f32 v127, v46, v47\n"
    "buffer_store_dwordx4 v[124:127], v3, s[24:27], s52 offen offset:0 sc1\n"
    "v_pk_mul_f32 v[116:117], v[40:41], v[40:41]\n"
    "s_add_u32 m0, s89, 0\n"
    "s_nop 0\n"
    "buffer_load_dwordx4 v252, s[8:11], s19 offen lds\n"
    "v_pk_mul_f32 v[118:119], v[42:43], v[42:43]\n"
    "v_pk_mul_f32 v[120:121], v[48:49], v[48:49]\n"
    "v_pk_mul_f32 v[122:123], v[50:51], v[50:51]\n"
    "v_pk_fma_f32 v[116:117], v[116:117], v[12:13], v[14:15]\n"
    "v_pk_fma_f32 v[118:119], v[118:119], v[12:13], v[14:15]\n"
    "v_pk_fma_f32 v[120:121], v[120:121], v[12:13], v[14:15]\n"
    "v_pk_fma_f32 v[122:123], v[122:123], v[12:13], v[14:15]\n"
    "v_pk_mul_f32 v[116:117], v[116:117], v[40:41]\n"
    "s_add_u32 m0, s89, 1024\n"
    "s_nop 0\n"
    "buffer_load_dwordx4 v253, s[8:11], s19 offen lds\n"
    "v_pk_mul_f32 v[118:119], v[118:119], v[42:43]\n"
    "v_pk_mul_f32 v[120:121], v[120:121], v[48:49]\n"
    "v_pk_mul_f32 v[122:123], v[122:123], v[50:51]\n"
    "v_exp_f32 v116, v116\n"
    "v_exp_f32 v117, v117\n"
    "v_exp_f32 v118, v118\n"
    "v_exp_f32 v119, v119\n"
    "v_exp_f32 v120, v120\n"
    "s_add_u32 m0, s89, 2048\n"
    "s_nop 0\n"
    "buffer_load_dwordx4 v254, s[8:11], s19 offen lds\n"
    "v_exp_f32 v121, v121\n"
    "v_exp_f32 v122, v122\n"
    "v_exp_f32 v123, v123\n"
    "v_pk_fma_f32 v[116:117], v[116:117], v[16:17], v[16:17]\n"
    "v_pk_fma_f32 v[118:119], v[118:119], v[16:17], v[16:17]\n"
    "v_pk_fma_f32 v[120:121], v[120:121], v[16:17], v[16:17]\n"
    "v_pk_fma_f32 v[122:123], v[122:123], v[16:17], v[16:17]\n"
    "v_rcp_f32 v116, v116\n"
    "s_add_u32 m0, s89, 3072\n"
    "s_nop 0\n"
    "buffer_load_dwordx4 v255, s[56:59], s20 offen lds\n"
    "v_rcp_f32 v117, v117\n"
    "v_rcp_f32 v118, v118\n"
    "v_rcp_f32 v119, v119\n"
    "v_rcp_f32 v120, v120\n"
    "v_rcp_f32 v121, v121\n"
    "v_rcp_f32 v122, v122\n"
    "v_rcp_f32 v123, v123\n"
    "v_pk_mul_f32 v[40:41], v[40:41], v[116:117]\n"
    "s_add_u32 m0, s89, 4096\n"
    "s_nop 0\n"
    "buffer_load_dwordx4 v255, s[60:63], s20 offen lds\n"
    "v_pk_mul_f32 v[42:43], v[42:43], v[118:119]\n"
    "v_pk_mul_f32 v[48:49], v[48:49], v[120:121]\n"
    "v_pk_mul_f32 v[50:51], v[50:51], v[122:123]\n"
    "v_cvt_pk_bf16_f32 v128, v40, v41\n"
    "v_cvt_pk_bf16_f32 v129, v42, v43\n"
    "v_cvt_pk_bf16_f32 v130, v48, v49\n"
    "v_cvt_pk_bf16_f32 v131, v50, v51\n"
    "buffer_store_dwordx4 v[128:131], v3, s[24:27], s52 offen offset:1024 sc1\n"
    "v_pk_mul_f32 v[116:117], v[52:53], v[52:53]\n"
    "v_pk_mul_f32 v[118:119], v[54:55], v[54:55]\n"
    "v_pk_mul_f32 v[120:121], v[60:61], v[60:61]\n"
    "v_pk_mul_f32 v[122:123], v[62:63], v[62:63]\n"
    "v_pk_fma_f32 v[116:117], v[116:117], v[6:7], v[8:9]\n"
    "v_pk_fma_f32 v[118:119], v[118:119], v[6:7], v[8:9]\n"
    "v_pk_fma_f32 v[120:121], v[120:121], v[6:7], v[8:9]\n"
    "v_pk_fma_f32 v[122:123], v[122:123], v[6:7], v[8:9]\n"
    "v_pk_mul_f32 v[116:117], v[116:117], v[52:53]\n"
    "v_pk_mul_f32 v[118:119], v[118:119], v[54:55]\n"
    "v_pk_mul_f32 v[120:121], v[120:121], v[60:61]\n"
    "v_pk_mul_f32 v[122:123], v[122:123], v[62:63]\n"
    "v_exp_f32 v116, v116\n"
    "v_exp_f32 v117, v117\n"
    "v_exp_f32 v118, v118\n"
    "v_exp_f32 v119, v119\n"
    "v_exp_f32 v120, v120\n"
    "v_exp_f32 v121, v121\n"
    "v_exp_f32 v122, v122\n"
    "v_exp_f32 v123, v123\n"
    "v_pk_fma_f32 v[116:117], v[116:117], v[10:11], v[10:11]\n"
    "v_pk_fma_f32 v[118:119], v[118:119], v[10:11], v[10:11]\n"
    "v_pk_fma_f32 v[120:121], v[120:121], v[10:11], v[10:11]\n"
    "v_pk_fma_f32 v[122:123], v[122:123], v[10:11], v[10:11]\n"
    "v_rcp_f32 v116, v116\n"
    "v_rcp_f32 v117, v117\n"
    "v_rcp_f32 v118, v118\n"
    "v_rcp_f32 v119, v119\n"
    "v_rcp_f32 v120, v120\n"
    "v_rcp_f32 v121, v121\n"
    "v_rcp_f32 v122, v122\n"
    "v_rcp_f32 v123, v123\n"
    "v_pk_mul_f32 v[52:53], v[52:53], v[116:117]\n"
    "v_pk_mul_f32 v[54:55], v[54:55], v[118:119]\n"
    "v_pk_mul_f32 v[60:61], v[60:61], v[120:121]\n"
    "v_pk_mul_f32 v[62:63], v[62:63], v[122:123]\n"
    "v_cvt_pk_bf16_f32 v124, v52, v53\n"
    "v_cvt_pk_bf16_f32 v125, v54, v55\n"
    "v_cvt_pk_bf16_f32 v126, v60, v61\n"
    "v_cvt_pk_bf16_f32 v127, v62, v63\n"
    "buffer_store_dwordx4 v[124:127], v3, s[24:27], s53 offen offset:0 sc1\n"
    "v_pk_mul_f32 v[116:117], v[56:57], v[56:57]\n"
    "v_pk_mul_f32 v[118:119], v[58:59], v[58:59]\n"
    "v_pk_mul_f32 v[120:121], v[64:65], v[64:65]\n"
    "v_pk_mul_f32 v[122:123], v[66:67], v[66:67]\n"
    "v_pk_fma_f32 v[116:117], v[116:117], v[12:13], v[14:15]\n"
    "v_pk_fma_f32 v[118:119], v[118:119], v[12:13], v[14:15]\n"
    "v_pk_fma_f32 v[120:121], v[120:121], v[12:13], v[14:15]\n"
    "v_pk_fma_f32 v[122:123], v[122:123], v[12:13], v[14:15]\n"
    "v_pk_mul_f32 v[116:117], v[116:117], v[56:57]\n"
    "v_pk_mul_f32 v[118:119], v[118:119], v[58:59]\n"
    "v_pk_mul_f32 v[120:121], v[120:121], v[64:65]\n"
    "v_pk_mul_f32 v[122:123], v[122:123], v[66:67]\n"
    "v_exp_f32 v116, v116\n"
    "v_exp_f32 v117, v117\n"
    "v_exp_f32 v118, v118\n"
    "v_exp_f32 v119, v119\n"
    "v_exp_f32 v120, v120\n"
    "v_exp_f32 v121, v121\n"
    "v_exp_f32 v122, v122\n"
    "v_exp_f32 v123, v123\n"
    "v_pk_fma_f32 v[116:117], v[116:117], v[16:17], v[16:17]\n"
    "v_pk_fma_f32 v[118:119], v[118:119], v[16:17], v[16:17]\n"
    "v_pk_fma_f32 v[120:121], v[120:121], v[16:17], v[16:17]\n"
    "v_pk_fma_f32 v[122:123], v[122:123], v[16:17], v[16:17]\n"
    "v_rcp_f32 v116, v116\n"
    "v_rcp_f32 v117, v117\n"
    "v_rcp_f32 v118, v118\n"
    "v_rcp_f32 v119, v119\n"
    "v_rcp_f32 v120, v120\n"
    "v_rcp_f32 v121, v121\n"
    "v_rcp_f32 v122, v122\n"
    "v_rcp_f32 v123, v123\n"
    "v_pk_mul_f32 v[56:57], v[56:57], v[116:117]\n"
    "v_pk_mul_f32 v[58:59], v[58:59], v[118:119]\n"
    "v_pk_mul_f32 v[64:65], v[64:65], v[120:121]\n"
    "v_pk_mul_f32 v[66:67], v[66:67], v[122:123]\n"
    "v_cvt_pk_bf16_f32 v128, v56, v57\n"
    "v_cvt_pk_bf16_f32 v129, v58, v59\n"
    "v_cvt_pk_bf16_f32 v130, v64, v65\n"
    "v_cvt_pk_bf16_f32 v131, v66, v67\n"
    "buffer_store_dwordx4 v[128:131], v3, s[24:27], s53 offen offset:1024 sc1\n"
    "v_pk_mul_f32 v[116:117], v[68:69], v[68:69]\n"
    "v_pk_mul_f32 v[118:119], v[70:71], v[70:71]\n"
    "v_pk_mul_f32 v[120:121], v[76:77], v[76:77]\n"
    "v_pk_mul_f32 v[122:123], v[78:79], v[78:79]\n"
    "v_pk_fma_f32 v[116:117], v[116:117], v[6:7], v[8:9]\n"
    "v_pk_fma_f32 v[118:119], v[118:119], v[6:7], v[8:9]\n"
    "v_pk_fma_f32 v[120:121], v[120:121], v[6:7], v[8:9]\n"
    "v_pk_fma_f32 v[122:123], v[122:123], v[6:7], v[8:9]\n"
    "v_pk_mul_f32 v[116:117], v[116:117], v[68:69]\n"
    "v_pk_mul_f32 v[118:119], v[118:119], v[70:71]\n"
    "v_pk_mul_f32 v[120:121], v[120:121], v[76:77]\n"
    "v_pk_mul_f32 v[122:123], v[122:123], v[78:79]\n"
    "v_exp_f32 v116, v116\n"
    "v_exp_f32 v117, v117\n"
    "v_exp_f32 v118, v118\n"
    "v_exp_f32 v119, v119\n"
    "v_exp_f32 v120, v120\n"
    "v_exp_f32 v121, v121\n"
    "v_exp_f32 v122, v122\n"
    "v_exp_f32 v123, v123\n"
    "v_pk_fma_f32 v[116:117], v[116:117], v[10:11], v[10:11]\n"
    "v_pk_fma_f32 v[118:119], v[118:119], v[10:11], v[10:11]\n"
    "v_pk_fma_f32 v[120:121], v[120:121], v[10:11], v[10:11]\n"
    "v_pk_fma_f32 v[122:123], v[122:123], v[10:11], v[10:11]\n"
    "v_rcp_f32 v116, v116\n"
    "v_rcp_f32 v117, v117\n"
    "v_rcp_f32 v118, v118\n"
    "v_rcp_f32 v119, v119\n"
    "v_rcp_f32 v120, v120\n"
    "v_rcp_f32 v121, v121\n"
    "v_rcp_f32 v122, v122\n"
    "v_rcp_f32 v123, v123\n"
    "v_pk_mul_f32 v[68:69], v[68:69], v[116:117]\n"
    "v_pk_mul_f32 v[70:71], v[70:71], v[118:119]\n"
    "v_pk_mul_f32 v[76:77], v[76:77], v[120:121]\n"
    "v_pk_mul_f32 v[78:79], v[78:79], v[122:123]\n"
    "v_cvt_pk_bf16_f32 v124, v68, v69\n"
    "v_cvt_pk_bf16_f32 v125, v70, v71\n"
    "v_cvt_pk_bf16_f32 v126, v76, v77\n"
    "v_cvt_pk_bf16_f32 v127, v78, v79\n"
    "buffer_store_dwordx4 v[124:127], v3, s[24:27], s54 offen offset:0 sc1\n"
    "v_pk_mul_f32 v[116:117], v[72:73], v[72:73]\n"
    "v_pk_mul_f32 v[118:119], v[74:75], v[74:75]\n"
    "v_pk_mul_f32 v[120:121], v[80:81], v[80:81]\n"
    "v_pk_mul_f32 v[122:123], v[82:83], v[82:83]\n"
    "v_pk_fma_f32 v[116:117], v[116:117], v[12:13], v[14:15]\n"
    "v_pk_fma_f32 v[118:119], v[118:119], v[12:13], v[14:15]\n"
    "v_pk_fma_f32 v[120:121], v[120:121], v[12:13], v[14:15]\n"
    "v_pk_fma_f32 v[122:123], v[122:123], v[12:13], v[14:15]\n"
    "v_pk_mul_f32 v[116:117], v[116:117], v[72:73]\n"
    "v_pk_mul_f32 v[118:119], v[118:119], v[74:75]\n"
    "v_pk_mul_f32 v[120:121], v[120:121], v[80:81]\n"
    "v_pk_mul_f32 v[122:123], v[122:123], v[82:83]\n"
    "v_exp_f32 v116, v116\n"
    "v_exp_f32 v117, v117\n"
    "v_exp_f32 v118, v118\n"
    "v_exp_f32 v119, v119\n"
    "v_exp_f32 v120, v120\n"
    "v_exp_f32 v121, v121\n"
    "v_exp_f32 v122, v122\n"
    "v_exp_f32 v123, v123\n"
    "v_pk_fma_f32 v[116:117], v[116:117], v[16:17], v[16:17]\n"
    "v_pk_fma_f32 v[118:119], v[118:119], v[16:17], v[16:17]\n"
    "v_pk_fma_f32 v[120:121], v[120:121], v[16:17], v[16:17]\n"
    "v_pk_fma_f32 v[122:123], v[122:123], v[16:17], v[16:17]\n"
    "v_rcp_f32 v116, v116\n"
    "v_rcp_f32 v117, v117\n"
    "v_rcp_f32 v118, v118\n"
    "v_rcp_f32 v119, v119\n"
    "v_rcp_f32 v120, v120\n"
    "v_rcp_f32 v121, v121\n"
    "v_rcp_f32 v122, v122\n"
    "v_rcp_f32 v123, v123\n"
    "v_pk_mul_f32 v[72:73], v[72:73], v[116:117]\n"
    "v_pk_mul_f32 v[74:75], v[74:75], v[118:119]\n"
    "v_pk_mul_f32 v[80:81], v[80:81], v[120:121]\n"
    "v_pk_mul_f32 v[82:83], v[82:83], v[122:123]\n"
    "v_cvt_pk_bf16_f32 v128, v72, v73\n"
    "v_cvt_pk_bf16_f32 v129, v74, v75\n"
    "v_cvt_pk_bf16_f32 v130, v80, v81\n"
    "v_cvt_pk_bf16_f32 v131, v82, v83\n"
    "buffer_store_dwordx4 v[128:131], v3, s[24:27], s54 offen offset:1024 sc1\n"
    "s_mul_i32 s95, s79, 24\n"
    "s_mul_i32 s92, s68, 4\n"
    "s_add_u32 s95, s95, s92\n"
    "s_xor_b32 s92, s71, 1\n"
    "s_mul_i32 s92, s92, 88\n"
    "s_add_u32 s92, s92, s95\n"
    "s_lshl_b32 s92, s92, 12\n"
    "s_add_u32 s40, s92, 4194304\n"
    "s_add_u32 s41, s92, 4198400\n"
    "s_add_u32 s42, s92, 4202496\n"
    "s_add_u32 s43, s92, 4206592\n"
    "s_xor_b32 s92, s71, 0\n"
    "s_mul_i32 s92, s92, 88\n"
    "s_add_u32 s92, s92, s95\n"
    "s_lshl_b32 s92, s92, 12\n"
    "s_add_u32 s46, s92, 4194304\n"
    "s_add_u32 s47, s92, 4198400\n"
    "s_add_u32 s48, s92, 4202496\n"
    "s_add_u32 s49, s92, 4206592\n"
    "s_mul_i32 s95, s79, 20\n"
    "s_mul_i32 s92, s68, 2\n"
    "s_add_u32 s95, s95, s92\n"
    "s_xor_b32 s92, s71, 1\n"
    "s_mul_i32 s92, s92, 88\n"
    "s_add_u32 s92, s92, s95\n"
    "s_lshl_b32 s92, s92, 12\n"
    "s_add_u32 s44, s92, 4390912\n"
    "s_add_u32 s45, s92, 4395008\n"
    "s_xor_b32 s92, s71, 0\n"
    "s_mul_i32 s92, s92, 88\n"
    "s_add_u32 s92, s92, s95\n"
    "s_lshl_b32 s92, s92, 12\n"
    "s_add_u32 s50, s92, 4390912\n"
    "s_add_u32 s51, s92, 4395008\n"
    "s_lshl_b32 s92, s69, 4\n"
    "s_add_u32 s93, s92, s70\n"
    "s_lshl_b32 s93, s93, 4\n"
    "s_add_u32 s93, s93, s68\n"
    "s_lshl_b32 s93, s93, 2\n"
    "s_add_u32 s98, s93, 8192\n"
    "s_lshl_b32 s93, s79, 3\n"
    "s_add_u32 s92, s92, s93\n"
    "s_lshl_b32 s95, s68, 2\n"
    "s_lshr_b32 s93, 16912, s95\n"
    "s_and_b32 s93, s93, 15\n"
    "s_add_u32 s93, s93, s92\n"
    "s_lshl_b32 s93, s93, 6\n"
    "s_add_u32 s99, s93, 8192\n"
    "s_lshr_b32 s93, 8448, s95\n"
    "s_and_b32 s93, s93, 15\n"
    "s_add_u32 s93, s93, s92\n"
    "s_lshl_b32 s93, s93, 6\n"
    "s_add_u32 s34, s93, 24576\n"
    "s_lshl_b32 s92, s71, 3\n"
    "s_add_u32 s92, s92, s72\n"
    "s_lshl_b32 s92, s92, 2\n"
    "s_add_u32 s92, s92, s68\n"
    "s_lshl_b32 s92, s92, 12\n"
    "s_add_u32 s35, s92, 16777216\n"
    "s_add_u32 s92, s68, 0\n"
    "s_and_b32 s92, s92, 3\n"
    "s_lshl_b32 s92, s92, 10\n"
    "v_add_u32 v5, s92, v3\n"
    "s_add_u32 s92, s68, 1\n"
    "s_and_b32 s92, s92, 3\n"
    "s_lshl_b32 s92, s92, 10\n"
    "v_add_u32 v6, s92, v3\n"
    "s_add_u32 s92, s68, 2\n"
    "s_and_b32 s92, s92, 3\n"
    "s_lshl_b32 s92, s92, 10\n"
    "v_add_u32 v7, s92, v3\n"
    "s_add_u32 s92, s68, 3\n"
    "s_and_b32 s92, s92, 3\n"
    "s_lshl_b32 s92, s92, 10\n"
    "v_add_u32 v8, s92, v3\n"
    "v_bfe_u32 v15, v0, 2, 1\n"
    "v_and_b32 v14, 3, v0\n"
    "v_lshl_add_u32 v14, v15, 4, v14\n"
    "v_lshlrev_b32 v14, 2, v14\n"
    "v_lshlrev_b32 v10, 3, v2\n"
    "v_add_u32 v10, s89, v10\n"
    "v_mul_u32_u24 v9, 160, v1\n"
    "v_add_u32 v9, v9, v10\n"
    "s_add_u32 s92, s68, 1\n"
    "s_and_b32 s92, s92, 3\n"
    "s_mul_i32 s92, s92, 15360\n"
    "s_add_u32 s92, s92, 0\n"
    "v_add_u32 v11, s92, v3\n"
    "s_add_u32 s92, s68, 2\n"
    "s_and_b32 s92, s92, 3\n"
    "s_mul_i32 s92, s92, 15360\n"
    "s_add_u32 s92, s92, 5120\n"
    "v_add_u32 v12, s92, v3\n"
    "s_add_u32 s92, s68, 3\n"
    "s_and_b32 s92, s92, 3\n"
    "s_mul_i32 s92, s92, 15360\n"
    "s_add_u32 s92, s92, 10240\n"
    "v_add_u32 v13, s92, v3\n"
    "s_waitcnt vmcnt(0)\n"
    "s_mov_b64 exec, 1\n"
    "v_mov_b32 v30, s73\n"
    "v_mov_b32 v31, 0\n"
    "buffer_store_dword v30, v31, s[24:27], s98 offen sc1\n"
    "s_mov_b64 exec, -1\n"
    "ds_read_b64 v[16:17], v9 offset:0\n"
    "ds_read_b64 v[116:117], v10 offset:3072\n"
    "ds_read_b64 v[126:127], v10 offset:4096\n"
    "ds_read_b64 v[18:19], v9 offset:32\n"
    "ds_read_b64 v[118:119], v10 offset:3104\n"
    "ds_read_b64 v[128:129], v10 offset:4128\n"
    "ds_read_b64 v[20:21], v9 offset:64\n"
    "ds_read_b64 v[120:121], v10 offset:3136\n"
    "ds_read_b64 v[130:131], v10 offset:4160\n"
    "ds_read_b64 v[22:23], v9 offset:96\n"
    "ds_read_b64 v[122:123], v10 offset:3168\n"
    "ds_read_b64 v[132:133], v10 offset:4192\n"
    "ds_read_b64 v[24:25], v9 offset:128\n"
    "ds_read_b64 v[124:125], v10 offset:3200\n"
    "ds_read_b64 v[134:135], v10 offset:4224\n"
    "s_xor_b32 s92, s79, 1\n"
    "s_lshl_b32 s92, s92, 8\n"
    "s_lshl_b32 s93, s68, 6\n"
    "s_add_u32 s92, s92, s93\n"
    "v_lshl_add_u32 v26, v1, 2, s92\n"
    "v_add_u32 v26, 158976, v26\n"
    "ds_read_b32 v27, v26 offset:512\n"
    "ds_read_b32 v26, v26\n"
    "buffer_load_dwordx4 v[172:175], v3, s[24:27], s29 offen offset:0 sc1\n"
    "buffer_load_dwordx4 v[176:179], v3, s[24:27], s29 offen offset:1024 sc1\n"
    "buffer_load_dwordx4 v[180:183], v3, s[24:27], s29 offen offset:2048 sc1\n"
    "buffer_load_dwordx4 v[184:187], v3, s[24:27], s29 offen offset:3072 sc1\n"
    "s_add_u32 s95, s29, 4096\n"
    "buffer_load_dwordx4 v[188:191], v3, s[24:27], s95 offen sc1\n"
    "buffer_load_dwordx4 v[192:195], v3, s[24:27], s30 offen offset:0 sc1\n"
    "buffer_load_dwordx4 v[196:199], v3, s[24:27], s30 offen offset:1024 sc1\n"
    "buffer_load_dwordx4 v[200:203], v3, s[24:27], s30 offen offset:2048 sc1\n"
    "buffer_load_dwordx4 v[204:207], v3, s[24:27], s30 offen offset:3072 sc1\n"
    "s_add_u32 s95, s30, 4096\n"
    "buffer_load_dwordx4 v[208:211], v3, s[24:27], s95 offen sc1\n"
    "buffer_load_dwordx4 v[212:215], v3, s[24:27], s31 offen offset:0 sc1\n"
    "buffer_load_dwordx4 v[216:219], v3, s[24:27], s31 offen offset:1024 sc1\n"
    "buffer_load_dwordx4 v[220:223], v3, s[24:27], s31 offen offset:2048 sc1\n"
    "buffer_load_dwordx4 v[224:227], v3, s[24:27], s31 offen offset:3072 sc1\n"
    "s_add_u32 s95, s31, 4096\n"
    "buffer_load_dwordx4 v[228:231], v3, s[24:27], s95 offen sc1\n"
    "buffer_load_dwordx4 v[232:235], v3, s[24:27], s32 offen offset:0 sc1\n"
    "buffer_load_dwordx4 v[236:239], v3, s[24:27], s32 offen offset:1024 sc1\n"
    "buffer_load_dwordx4 v[240:243], v3, s[24:27], s32 offen offset:2048 sc1\n"
    "buffer_load_dwordx4 v[244:247], v3, s[24:27], s32 offen offset:3072 sc1\n"
    "s_add_u32 s95, s32, 4096\n"
    "buffer_load_dwordx4 v[248:251], v3, s[24:27], s95 offen sc1\n"
    "buffer_load_dwordx4 v[252:255], v3, s[24:27], s33 offen offset:0 sc1\n"
    "buffer_load_dwordx4 v[136:139], v3, s[24:27], s33 offen offset:1024 sc1\n"
    "buffer_load_dwordx4 v[140:143], v3, s[24:27], s33 offen offset:2048 sc1\n"
    "buffer_load_dwordx4 v[144:147], v3, s[24:27], s33 offen offset:3072 sc1\n"
    "s_add_u32 s95, s33, 4096\n"
    "buffer_load_dwordx4 v[148:151], v3, s[24:27], s95 offen sc1\n"
    "buffer_load_dword v15, v14, s[24:27], s99 offen sc1\n"
    "s_waitcnt lgkmcnt(14)\n"
    "v_lshlrev_b32 v28, 16, v16\n"
    "s_waitcnt lgkmcnt(14)\n"
    "v_lshlrev_b32 v29, 16, v116\n"
    "s_waitcnt lgkmcnt(14)\n"
    "v_lshlrev_b32 v36, 16, v126\n"
    "v_add_f32 v36, v36, v28\n"
    "s_waitcnt lgkmcnt(0)\n"
    "v_sub_f32 v28, v28, v26\n"
    "v_mul_f32 v29, v29, v27\n"
    "v_fmac_f32 v36, v28, v29\n"
    "v_and_b32 v30, s1, v16\n"
    "v_and_b32 v31, s1, v116\n"
    "v_and_b32 v37, s1, v126\n"
    "v_add_f32 v37, v37, v30\n"
    "v_sub_f32 v30, v30, v26\n"
    "v_mul_f32 v31, v31, v27\n"
    "v_fmac_f32 v37, v30, v31\n"
    "v_lshlrev_b32 v28, 16, v17\n"
    "v_lshlrev_b32 v29, 16, v117\n"
    "v_lshlrev_b32 v38, 16, v127\n"
    "v_add_f32 v38, v38, v28\n"
    "v_sub_f32 v28, v28, v26\n"
    "v_mul_f32 v29, v29, v27\n"
    "v_fmac_f32 v38, v28, v29\n"
    "v_and_b32 v30, s1, v17\n"
    "v_and_b32 v31, s1, v117\n"
    "v_and_b32 v39, s1, v127\n"
    "v_add_f32 v39, v39, v30\n"
    "v_sub_f32 v30, v30, v26\n"
    "v_mul_f32 v31, v31, v27\n"
    "v_fmac_f32 v39, v30, v31\n"
    "v_lshlrev_b32 v28, 16, v18\n"
    "v_lshlrev_b32 v29, 16, v118\n"
    "v_lshlrev_b32 v40, 16, v128\n"
    "v_add_f32 v40, v40, v28\n"
    "v_sub_f32 v28, v28, v26\n"
    "v_mul_f32 v29, v29, v27\n"
    "v_fmac_f32 v40, v28, v29\n"
    "v_and_b32 v30, s1, v18\n"
    "v_and_b32 v31, s1, v118\n"
    "v_and_b32 v41, s1, v128\n"
    "v_add_f32 v41, v41, v30\n"
    "v_sub_f32 v30, v30, v26\n"
    "v_mul_f32 v31, v31, v27\n"
    "v_fmac_f32 v41, v30, v31\n"
    "v_lshlrev_b32 v28, 16, v19\n"
    "v_lshlrev_b32 v29, 16, v119\n"
    "v_lshlrev_b32 v42, 16, v129\n"
    "v_add_f32 v42, v42, v28\n"
    "v_sub_f32 v28, v28, v26\n"
    "v_mul_f32 v29, v29, v27\n"
    "v_fmac_f32 v42, v28, v29\n"
    "v_and_b32 v30, s1, v19\n"
    "v_and_b32 v31, s1, v119\n"
    "v_and_b32 v43, s1, v129\n"
    "v_add_f32 v43, v43, v30\n"
    "v_sub_f32 v30, v30, v26\n"
    "v_mul_f32 v31, v31, v27\n"
    "v_fmac_f32 v43, v30, v31\n"
    "v_lshlrev_b32 v28, 16, v20\n"
    "v_lshlrev_b32 v29, 16, v120\n"
    "v_lshlrev_b32 v44, 16, v130\n"
    "v_add_f32 v44, v44, v28\n"
    "v_sub_f32 v28, v28, v26\n"
    "v_mul_f32 v29, v29, v27\n"
    "v_fmac_f32 v44, v28, v29\n"
    "v_and_b32 v30, s1, v20\n"
    "v_and_b32 v31, s1, v120\n"
    "v_and_b32 v45, s1, v130\n"
    "v_add_f32 v45, v45, v30\n"
    "v_sub_f32 v30, v30, v26\n"
    "v_mul_f32 v31, v31, v27\n"
    "v_fmac_f32 v45, v30, v31\n"
    "v_lshlrev_b32 v28, 16, v21\n"
    "v_lshlrev_b32 v29, 16, v121\n"
    "v_lshlrev_b32 v46, 16, v131\n"
    "v_add_f32 v46, v46, v28\n"
    "v_sub_f32 v28, v28, v26\n"
    "v_mul_f32 v29, v29, v27\n"
    "v_fmac_f32 v46, v28, v29\n"
    "v_and_b32 v30, s1, v21\n"
    "v_and_b32 v31, s1, v121\n"
    "v_and_b32 v47, s1, v131\n"
    "v_add_f32 v47, v47, v30\n"
    "v_sub_f32 v30, v30, v26\n"
    "v_mul_f32 v31, v31, v27\n"
    "v_fmac_f32 v47, v30, v31\n"
    "v_lshlrev_b32 v28, 16, v22\n"
    "v_lshlrev_b32 v29, 16, v122\n"
    "v_lshlrev_b32 v48, 16, v132\n"
    "v_add_f32 v48, v48, v28\n"
    "v_sub_f32 v28, v28, v26\n"
    "v_mul_f32 v29, v29, v27\n"
    "v_fmac_f32 v48, v28, v29\n"
    "v_and_b32 v30, s1, v22\n"
    "v_and_b32 v31, s1, v122\n"
    "v_and_b32 v49, s1, v132\n"
    "v_add_f32 v49, v49, v30\n"
    "v_sub_f32 v30, v30, v26\n"
    "v_mul_f32 v31, v31, v27\n"
    "v_fmac_f32 v49, v30, v31\n"
    "v_lshlrev_b32 v28, 16, v23\n"
    "v_lshlrev_b32 v29, 16, v123\n"
    "v_lshlrev_b32 v50, 16, v133\n"
    "v_add_f32 v50, v50, v28\n"
    "v_sub_f32 v28, v28, v26\n"
    "v_mul_f32 v29, v29, v27\n"
    "v_fmac_f32 v50, v28, v29\n"
    "v_and_b32 v30, s1, v23\n"
    "v_and_b32 v31, s1, v123\n"
    "v_and_b32 v51, s1, v133\n"
    "v_add_f32 v51, v51, v30\n"
    "v_sub_f32 v30, v30, v26\n"
    "v_mul_f32 v31, v31, v27\n"
    "v_fmac_f32 v51, v30, v31\n"
    "v_lshlrev_b32 v28, 16, v24\n"
    "v_lshlrev_b32 v29, 16, v124\n"
    "v_lshlrev_b32 v52, 16, v134\n"
    "v_add_f32 v52, v52, v28\n"
    "v_sub_f32 v28, v28, v26\n"
    "v_mul_f32 v29, v29, v27\n"
    "v_fmac_f32 v52, v28, v29\n"
    "v_and_b32 v30, s1, v24\n"
    "v_and_b32 v31, s1, v124\n"
    "v_and_b32 v53, s1, v134\n"
    "v_add_f32 v53, v53, v30\n"
    "v_sub_f32 v30, v30, v26\n"
    "v_mul_f32 v31, v31, v27\n"
    "v_fmac_f32 v53, v30, v31\n"
    "v_lshlrev_b32 v28, 16, v25\n"
    "v_lshlrev_b32 v29, 16, v125\n"
    "v_lshlrev_b32 v54, 16, v135\n"
    "v_add_f32 v54, v54, v28\n"
    "v_sub_f32 v28, v28, v26\n"
    "v_mul_f32 v29, v29, v27\n"
    "v_fmac_f32 v54, v28, v29\n"
    "v_and_b32 v30, s1, v25\n"
    "v_and_b32 v31, s1, v125\n"
    "v_and_b32 v55, s1, v135\n"
    "v_add_f32 v55, v55, v30\n"
    "v_sub_f32 v30, v30, v26\n"
    "v_mul_f32 v31, v31, v27\n"
    "v_fmac_f32 v55, v30, v31\n"
    "s_mov_b32 s0, 131072\n"
    "s_nop 4\n"
    "s_branch Lk_chk_74\n"
    "Lk_poll_72:\n"
    "buffer_load_dword v15, v14, s[24:27], s99 offen sc1\n"
    "Lk_chk_74:\n"
    "s_waitcnt vmcnt(0)\n"
    "v_cmp_ne_u32 vcc, s73, v15\n"
    "s_nop 4\n"
    "s_cbranch_vccz Lk_done_73\n"
    "s_sleep 4\n"
    "s_sub_u32 s0, s0, 1\n"
    "s_cbranch_scc0 Lk_poll_72\n"
    "Lk_done_73:\n"
    "s_setprio 1\n"
    "buffer_load_dwordx4 v[16:19], v5, s[24:27], s40 offen sc1\n"
    "buffer_load_dwordx4 v[20:23], v6, s[24:27], s40 offen sc1\n"
    "buffer_load_dwordx4 v[24:27], v7, s[24:27], s40 offen sc1\n"
    "buffer_load_dwordx4 v[28:31], v8, s[24:27], s40 offen sc1\n"
    "buffer_load_dwordx4 v[32:35], v5, s[24:27], s41 offen sc1\n"
    "buffer_load_dwordx4 v[116:119], v6, s[24:27], s41 offen sc1\n"
    "buffer_load_dwordx4 v[120:123], v7, s[24:27], s41 offen sc1\n"
    "buffer_load_dwordx4 v[124:127], v8, s[24:27], s41 offen sc1\n"
    "buffer_load_dwordx4 v[128:131], v5, s[24:27], s42 offen sc1\n"
    "buffer_load_dwordx4 v[132:135], v6, s[24:27], s42 offen sc1\n"
    "s_waitcnt vmcnt(9)\n"
    "v_mfma_f32_16x16x32_bf16 v[36:39], v[152:155], v[16:19], v[36:39]\n"
    "v_mfma_f32_16x16x32_bf16 v[40:43], v[156:159], v[16:19], v[40:43]\n"
    "v_mfma_f32_16x16x32_bf16 v[44:47], v[160:163], v[16:19], v[44:47]\n"
    "v_mfma_f32_16x16x32_bf16 v[48:51], v[164:167], v[16:19], v[48:51]\n"
    "v_mfma_f32_16x16x32_bf16 v[52:55], v[168:171], v[16:19], v[52:55]\n"
    "buffer_load_dwordx4 v[16:19], v7, s[24:27], s42 offen sc1\n"
    "s_waitcnt vmcnt(9)\n"
    "v_mfma_f32_16x16x32_bf16 v[56:59], v[152:155], v[20:23], 0\n"
    "v_mfma_f32_16x16x32_bf16 v[60:63], v[156:159], v[20:23], 0\n"
    "v_mfma_f32_16x16x32_bf16 v[64:67], v[160:163], v[20:23], 0\n"
    "v_mfma_f32_16x16x32_bf16 v[68:71], v[164:167], v[20:23], 0\n"
    "v_mfma_f32_16x16x32_bf16 v[72:75], v[168:171], v[20:23], 0\n"
    "buffer_load_dwordx4 v[20:23], v8, s[24:27], s42 offen sc1\n"
    "s_waitcnt vmcnt(9)\n"
    "v_mfma_f32_16x16x32_bf16 v[76:79], v[152:155], v[24:27], 0\n"
    "v_mfma_f32_16x16x32_bf16 v[80:83], v[156:159], v[24:27], 0\n"
    "v_mfma_f32_16x16x32_bf16 v[84:87], v[160:163], v[24:27], 0\n"
    "v_mfma_f32_16x16x32_bf16 v[88:91], v[164:167], v[24:27], 0\n"
    "v_mfma_f32_16x16x32_bf16 v[92:95], v[168:171], v[24:27], 0\n"
    "buffer_load_dwordx4 v[24:27], v5, s[24:27], s43 offen sc1\n"
    "s_waitcnt vmcnt(9)\n"
    "v_mfma_f32_16x16x32_bf16 v[96:99], v[152:155], v[28:31], 0\n"
    "v_mfma_f32_16x16x32_bf16 v[100:103], v[156:159], v[28:31], 0\n"
    "v_mfma_f32_16x16x32_bf16 v[104:107], v[160:163], v[28:31], 0\n"
    "v_mfma_f32_16x16x32_bf16 v[108:111], v[164:167], v[28:31], 0\n"
    "v_mfma_f32_16x16x32_bf16 v[112:115], v[168:171], v[28:31], 0\n"
    "buffer_load_dwordx4 v[28:31], v6, s[24:27], s43 offen sc1\n"
    "s_waitcnt vmcnt(9)\n"
    "v_mfma_f32_16x16x32_bf16 v[36:39], v[172:175], v[32:35], v[36:39]\n"
    "v_mfma_f32_16x16x32_bf16 v[40:43], v[176:179], v[32:35], v[40:43]\n"
    "v_mfma_f32_16x16x32_bf16 v[44:47], v[180:183], v[32:35], v[44:47]\n"
    "v_mfma_f32_16x16x32_bf16 v[48:51], v[184:187], v[32:35], v[48:51]\n"
    "v_mfma_f32_16x16x32_bf16 v[52:55], v[188:191], v[32:35], v[52:55]\n"
    "buffer_load_dwordx4 v[32:35], v7, s[24:27], s43 offen sc1\n"
    "s_waitcnt vmcnt(9)\n"
    "v_mfma_f32_16x16x32_bf16 v[56:59], v[172:175], v[116:119], v[56:59]\n"
    "v_mfma_f32_16x16x32_bf16 v[60:63], v[176:179], v[116:119], v[60:63]\n"
    "v_mfma_f32_16x16x32_bf16 v[64:67], v[180:183], v[116:119], v[64:67]\n"
    "v_mfma_f32_16x16x32_bf16 v[68:71], v[184:187], v[116:119], v[68:71]\n"
    "v_mfma_f32_16x16x32_bf16 v[72:75], v[188:191], v[116:119], v[72:75]\n"
    "buffer_load_dwordx4 v[116:119], v8, s[24:27], s43 offen sc1\n"
    "buffer_load_dword v15, v14, s[24:27], s34 offen sc1\n"
    "s_waitcnt vmcnt(10)\n"
    "v_mfma_f32_16x16x32_bf16 v[76:79], v[172:175], v[120:123], v[76:79]\n"
    "v_mfma_f32_16x16x32_bf16 v[80:83], v[176:179], v[120:123], v[80:83]\n"
    "v_mfma_f32_16x16x32_bf16 v[84:87], v[180:183], v[120:123], v[84:87]\n"
    "v_mfma_f32_16x16x32_bf16 v[88:91], v[184:187], v[120:123], v[88:91]\n"
    "v_mfma_f32_16x16x32_bf16 v[92:95], v[188:191], v[120:123], v[92:95]\n"
    "s_waitcnt vmcnt(9)\n"
    "v_mfma_f32_16x16x32_bf16 v[96:99], v[172:175], v[124:127], v[96:99]\n"
    "v_mfma_f32_16x16x32_bf16 v[100:103], v[176:179], v[124:127], v[100:103]\n"
    "v_mfma_f32_16x16x32_bf16 v[104:107], v[180:183], v[124:127], v[104:107]\n"
    "v_mfma_f32_16x16x32_bf16 v[108:111], v[184:187], v[124:127], v[108:111]\n"
    "v_mfma_f32_16x16x32_bf16 v[112:115], v[188:191], v[124:127], v[112:115]\n"
    "s_waitcnt vmcnt(8)\n"
    "v_mfma_f32_16x16x32_bf16 v[36:39], v[192:195], v[128:131], v[36:39]\n"
    "v_mfma_f32_16x16x32_bf16 v[40:43], v[196:199], v[128:131], v[40:43]\n"
    "v_mfma_f32_16x16x32_bf16 v[44:47], v[200:203], v[128:131], v[44:47]\n"
    "v_mfma_f32_16x16x32_bf16 v[48:51], v[204:207], v[128:131], v[48:51]\n"
    "v_mfma_f32_16x16x32_bf16 v[52:55], v[208:211], v[128:131], v[52:55]\n"
    "s_waitcnt vmcnt(7)\n"
    "v_mfma_f32_16x16x32_bf16 v[56:59], v[192:195], v[132:135], v[56:59]\n"
    "v_mfma_f32_16x16x32_bf16 v[60:63], v[196:199], v[132:135], v[60:63]\n"
    "v_mfma_f32_16x16x32_bf16 v[64:67], v[200:203], v[132:135], v[64:67]\n"
    "v_mfma_f32_16x16x32_bf16 v[68:71], v[204:207], v[132:135], v[68:71]\n"
    "v_mfma_f32_16x16x32_bf16 v[72:75], v[208:211], v[132:135], v[72:75]\n"
    "s_waitcnt vmcnt(6)\n"
    "v_mfma_f32_16x16x32_bf16 v[76:79], v[192:195], v[16:19], v[76:79]\n"
    "v_mfma_f32_16x16x32_bf16 v[80:83], v[196:199], v[16:19], v[80:83]\n"
    "v_mfma_f32_16x16x32_bf16 v[84:87], v[200:203], v[16:19], v[84:87]\n"
    "v_mfma_f32_16x16x32_bf16 v[88:91], v[204:207], v[16:19], v[88:91]\n"
    "v_mfma_f32_16x16x32_bf16 v[92:95], v[208:211], v[16:19], v[92:95]\n"
    "s_waitcnt vmcnt(5)\n"
    "v_mfma_f32_16x16x32_bf16 v[96:99], v[192:195], v[20:23], v[96:99]\n"
    "v_mfma_f32_16x16x32_bf16 v[100:103], v[196:199], v[20:23], v[100:103]\n"
    "v_mfma_f32_16x16x32_bf16 v[104:107], v[200:203], v[20:23], v[104:107]\n"
    "v_mfma_f32_16x16x32_bf16 v[108:111], v[204:207], v[20:23], v[108:111]\n"
    "v_mfma_f32_16x16x32_bf16 v[112:115], v[208:211], v[20:23], v[112:115]\n"
    "s_waitcnt vmcnt(4)\n"
    "v_mfma_f32_16x16x32_bf16 v[36:39], v[212:215], v[24:27], v[36:39]\n"
    "v_mfma_f32_16x16x32_bf16 v[40:43], v[216:219], v[24:27], v[40:43]\n"
    "v_mfma_f32_16x16x32_bf16 v[44:47], v[220:223], v[24:27], v[44:47]\n"
    "v_mfma_f32_16x16x32_bf16 v[48:51], v[224:227], v[24:27], v[48:51]\n"
    "v_mfma_f32_16x16x32_bf16 v[52:55], v[228:231], v[24:27], v[52:55]\n"
    "s_waitcnt vmcnt(3)\n"
    "v_mfma_f32_16x16x32_bf16 v[56:59], v[212:215], v[28:31], v[56:59]\n"
    "v_mfma_f32_16x16x32_bf16 v[60:63], v[216:219], v[28:31], v[60:63]\n"
    "v_mfma_f32_16x16x32_bf16 v[64:67], v[220:223], v[28:31], v[64:67]\n"
    "v_mfma_f32_16x16x32_bf16 v[68:71], v[224:227], v[28:31], v[68:71]\n"
    "v_mfma_f32_16x16x32_bf16 v[72:75], v[228:231], v[28:31], v[72:75]\n"
    "s_waitcnt vmcnt(0)\n"
    "v_cmp_ne_u32 vcc, s73, v15\n"
    "s_nop 4\n"
    "s_cbranch_vccz Lk_skip_75\n"
    "s_mov_b32 s0, 131072\n"
    "s_nop 4\n"
    "Lk_poll_76:\n"
    "buffer_load_dword v15, v14, s[24:27], s34 offen sc1\n"
    "Lk_chk_78:\n"
    "s_waitcnt vmcnt(0)\n"
    "v_cmp_ne_u32 vcc, s73, v15\n"
    "s_nop 4\n"
    "s_cbranch_vccz Lk_done_77\n"
    "s_sleep 4\n"
    "s_sub_u32 s0, s0, 1\n"
    "s_cbranch_scc0 Lk_poll_76\n"
    "Lk_done_77:\n"
    "Lk_skip_75:\n"
    "buffer_load_dwordx4 v[120:123], v6, s[24:27], s44 offen sc1\n"
    "buffer_load_dwordx4 v[124:127], v6, s[24:27], s45 offen sc1\n"
    "buffer_load_dwordx4 v[128:131], v7, s[24:27], s44 offen sc1\n"
    "buffer_load_dwordx4 v[132:135], v7, s[24:27], s45 offen sc1\n"
    "buffer_load_dwordx4 v[16:19], v8, s[24:27], s44 offen sc1\n"
    "buffer_load_dwordx4 v[20:23], v8, s[24:27], s45 offen sc1\n"
    "buffer_load_dwordx4 v[24:27], v5, s[24:27], s44 offen sc1\n"
    "buffer_load_dwordx4 v[28:31], v5, s[24:27], s45 offen sc1\n"
    "v_mfma_f32_16x16x32_bf16 v[76:79], v[212:215], v[32:35], v[76:79]\n"
    "v_mfma_f32_16x16x32_bf16 v[80:83], v[216:219], v[32:35], v[80:83]\n"
    "v_mfma_f32_16x16x32_bf16 v[84:87], v[220:223], v[32:35], v[84:87]\n"
    "v_mfma_f32_16x16x32_bf16 v[88:91], v[224:227], v[32:35], v[88:91]\n"
    "v_mfma_f32_16x16x32_bf16 v[92:95], v[228:231], v[32:35], v[92:95]\n"
    "buffer_load_dwordx4 v[32:35], v5, s[24:27], s46 offen sc1\n"
    "v_mfma_f32_16x16x32_bf16 v[96:99], v[212:215], v[116:119], v[96:99]\n"
    "v_mfma_f32_16x16x32_bf16 v[100:103], v[216:219], v[116:119], v[100:103]\n"
    "v_mfma_f32_16x16x32_bf16 v[104:107], v[220:223], v[116:119], v[104:107]\n"
    "v_mfma_f32_16x16x32_bf16 v[108:111], v[224:227], v[116:119], v[108:111]\n"
    "v_mfma_f32_16x16x32_bf16 v[112:115], v[228:231], v[116:119], v[112:115]\n"
    "buffer_load_dwordx4 v[116:119], v6, s[24:27], s46 offen sc1\n"
    "s_waitcnt vmcnt(9)\n"
    "v_mfma_f32_16x16x32_bf16 v[56:59], v[232:235], v[120:123], v[56:59]\n"
    "v_mfma_f32_16x16x32_bf16 v[60:63], v[236:239], v[120:123], v[60:63]\n"
    "v_mfma_f32_16x16x32_bf16 v[64:67], v[240:243], v[120:123], v[64:67]\n"
    "v_mfma_f32_16x16x32_bf16 v[68:71], v[244:247], v[120:123], v[68:71]\n"
    "v_mfma_f32_16x16x32_bf16 v[72:75], v[248:251], v[120:123], v[72:75]\n"
    "buffer_load_dwordx4 v[120:123], v7, s[24:27], s46 offen sc1\n"
    "s_waitcnt vmcnt(9)\n"
    "v_mfma_f32_16x16x32_bf16 v[56:59], v[252:255], v[124:127], v[56:59]\n"
    "v_mfma_f32_16x16x32_bf16 v[60:63], v[136:139], v[124:127], v[60:63]\n"
    "v_mfma_f32_16x16x32_bf16 v[64:67], v[140:143], v[124:127], v[64:67]\n"
    "v_mfma_f32_16x16x32_bf16 v[68:71], v[144:147], v[124:127], v[68:71]\n"
    "v_mfma_f32_16x16x32_bf16 v[72:75], v[148:151], v[124:127], v[72:75]\n"
    "buffer_load_dwordx4 v[124:127], v8, s[24:27], s46 offen sc1\n"
    "s_waitcnt vmcnt(9)\n"
    "v_mfma_f32_16x16x32_bf16 v[76:79], v[232:235], v[128:131], v[76:79]\n"
    "v_mfma_f32_16x16x32_bf16 v[80:83], v[236:239], v[128:131], v[80:83]\n"
    "ds_write_b128 v11, v[56:59] offset:0\n"
    "v_mfma_f32_16x16x32_bf16 v[84:87], v[240:243], v[128:131], v[84:87]\n"
    "ds_write_b128 v11, v[60:63] offset:1024\n"
    "v_mfma_f32_16x16x32_bf16 v[88:91], v[244:247], v[128:131], v[88:91]\n"
    "ds_write_b128 v11, v[64:67] offset:2048\n"
    "v_mfma_f32_16x16x32_bf16 v[92:95], v[248:251], v[128:131], v[92:95]\n"
    "ds_write_b128 v11, v[68:71] offset:3072\n"
    "buffer_load_dwordx4 v[128:131], v5, s[24:27], s47 offen sc1\n"
    "s_waitcnt vmcnt(9)\n"
    "v_mfma_f32_16x16x32_bf16 v[76:79], v[252:255], v[132:135], v[76:79]\n"
    "v_mfma_f32_16x16x32_bf16 v[80:83], v[136:139], v[132:135], v[80:83]\n"
    "v_mfma_f32_16x16x32_bf16 v[84:87], v[140:143], v[132:135], v[84:87]\n"
    "v_mfma_f32_16x16x32_bf16 v[88:91], v[144:147], v[132:135], v[88:91]\n"
    "v_mfma_f32_16x16x32_bf16 v[92:95], v[148:151], v[132:135], v[92:95]\n"
    "ds_write_b128 v11, v[72:75] offset:4096\n"
    "buffer_load_dwordx4 v[132:135], v6, s[24:27], s47 offen sc1\n"
    "s_waitcnt vmcnt(9)\n"
    "v_mfma_f32_16x16x32_bf16 v[96:99], v[232:235], v[16:19], v[96:99]\n"
    "v_mfma_f32_16x16x32_bf16 v[100:103], v[236:239], v[16:19], v[100:103]\n"
    "ds_write_b128 v12, v[76:79] offset:0\n"
    "v_mfma_f32_16x16x32_bf16 v[104:107], v[240:243], v[16:19], v[104:107]\n"
    "ds_write_b128 v12, v[80:83] offset:1024\n"
    "v_mfma_f32_16x16x32_bf16 v[108:111], v[244:247], v[16:19], v[108:111]\n"
    "ds_write_b128 v12, v[84:87] offset:2048\n"
    "v_mfma_f32_16x16x32_bf16 v[112:115], v[248:251], v[16:19], v[112:115]\n"
    "ds_write_b128 v12, v[88:91] offset:3072\n"
    "buffer_load_dwordx4 v[16:19], v7, s[24:27], s47 offen sc1\n"
    "s_waitcnt vmcnt(9)\n"
    "v_mfma_f32_16x16x32_bf16 v[96:99], v[252:255], v[20:23], v[96:99]\n"
    "v_mfma_f32_16x16x32_bf16 v[100:103], v[136:139], v[20:23], v[100:103]\n"
    "v_mfma_f32_16x16x32_bf16 v[104:107], v[140:143], v[20:23], v[104:107]\n"
    "v_mfma_f32_16x16x32_bf16 v[108:111], v[144:147], v[20:23], v[108:111]\n"
    "v_mfma_f32_16x16x32_bf16 v[112:115], v[148:151], v[20:23], v[112:115]\n"
    "ds_write_b128 v12, v[92:95] offset:4096\n"
    "buffer_load_dwordx4 v[20:23], v8, s[24:27], s47 offen sc1\n"
    "s_waitcnt vmcnt(9)\n"
    "v_mfma_f32_16x16x32_bf16 v[36:39], v[232:235], v[24:27], v[36:39]\n"
    "ds_write_b128 v13, v[96:99] offset:0\n"
    "v_mfma_f32_16x16x32_bf16 v[40:43], v[236:239], v[24:27], v[40:43]\n"
    "ds_write_b128 v13, v[100:103] offset:1024\n"
    "v_mfma_f32_16x16x32_bf16 v[44:47], v[240:243], v[24:27], v[44:47]\n"
    "ds_write_b128 v13, v[104:107] offset:2048\n"
    "v_mfma_f32_16x16x32_bf16 v[48:51], v[244:247], v[24:27], v[48:51]\n"
    "ds_write_b128 v13, v[108:111] offset:3072\n"
    "v_mfma_f32_16x16x32_bf16 v[52:55], v[248:251], v[24:27], v[52:55]\n"
    "ds_write_b128 v13, v[112:115] offset:4096\n"
    "buffer_load_dwordx4 v[24:27], v5, s[24:27], s48 offen sc1\n"
    "s_waitcnt vmcnt(9)\n"
    "v_mfma_f32_16x16x32_bf16 v[36:39], v[252:255], v[28:31], v[36:39]\n"
    "v_mfma_f32_16x16x32_bf16 v[40:43], v[136:139], v[28:31], v[40:43]\n"
    "v_mfma_f32_16x16x32_bf16 v[44:47], v[140:143], v[28:31], v[44:47]\n"
    "v_mfma_f32_16x16x32_bf16 v[48:51], v[144:147], v[28:31], v[48:51]\n"
    "v_mfma_f32_16x16x32_bf16 v[52:55], v[148:151], v[28:31], v[52:55]\n"
    "buffer_load_dwordx4 v[28:31], v6, s[24:27], s48 offen sc1\n"
    "s_mov_b64 exec, 1\n"
    "v_mov_b32 v14, 161036\n"
    "v_mov_b32 v15, 1\n"
    "ds_add_u32 v14, v15\n"
    "s_mov_b64 exec, -1\n"
    "v_mov_b32 v14, 161036\n"
    "s_mov_b32 s0, 524288\n"
    "s_waitcnt lgkmcnt(0)\n"
    "Lk_lw_79:\n"
    "ds_read_b32 v15, v14\n"
    "s_waitcnt lgkmcnt(0)\n"
    "v_readfirstlane_b32 s92, v15\n"
    "s_nop 1\n"
    "s_cmp_ge_u32 s92, 4\n"
    "s_cbranch_scc1 Lk_lwd_80\n"
    "s_sub_u32 s0, s0, 1\n"
    "s_cbranch_scc0 Lk_lw_79\n"
    "Lk_lwd_80:\n"
    "s_mul_i32 s92, s68, 15360\n"
    "s_add_u32 s92, s92, 0\n"
    "v_add_u32 v14, s92, v3\n"
    "ds_read_b128 v[56:59], v14 offset:0\n"
    "ds_read_b128 v[60:63], v14 offset:1024\n"
    "ds_read_b128 v[64:67], v14 offset:2048\n"
    "ds_read_b128 v[68:71], v14 offset:3072\n"
    "ds_read_b128 v[72:75], v14 offset:4096\n"
    "ds_read_b128 v[76:79], v14 offset:5120\n"
    "ds_read_b128 v[80:83], v14 offset:6144\n"
    "ds_read_b128 v[84:87], v14 offset:7168\n"
    "ds_read_b128 v[88:91], v14 offset:8192\n"
    "ds_read_b128 v[92:95], v14 offset:9216\n"
    "ds_read_b128 v[96:99], v14 offset:10240\n"
    "ds_read_b128 v[100:103], v14 offset:11264\n"
    "ds_read_b128 v[104:107], v14 offset:12288\n"
    "ds_read_b128 v[108:111], v14 offset:13312\n"
    "ds_read_b128 v[112:115], v14 offset:14336\n"
    "s_waitcnt lgkmcnt(14)\n"
    "v_add_f32 v36, v36, v56\n"
    "v_add_f32 v37, v37, v57\n"
    "v_add_f32 v38, v38, v58\n"
    "v_add_f32 v39, v39, v59\n"
    "s_waitcnt lgkmcnt(13)\n"
    "v_add_f32 v40, v40, v60\n"
    "v_add_f32 v41, v41, v61\n"
    "v_add_f32 v42, v42, v62\n"
    "v_add_f32 v43, v43, v63\n"
    "s_waitcnt lgkmcnt(12)\n"
    "v_add_f32 v44, v44, v64\n"
    "v_add_f32 v45, v45, v65\n"
    "v_add_f32 v46, v46, v66\n"
    "v_add_f32 v47, v47, v67\n"
    "s_waitcnt lgkmcnt(11)\n"
    "v_add_f32 v48, v48, v68\n"
    "v_add_f32 v49, v49, v69\n"
    "v_add_f32 v50, v50, v70\n"
    "v_add_f32 v51, v51, v71\n"
    "s_waitcnt lgkmcnt(10)\n"
    "v_add_f32 v52, v52, v72\n"
    "v_add_f32 v53, v53, v73\n"
    "v_add_f32 v54, v54, v74\n"
    "v_add_f32 v55, v55, v75\n"
    "s_waitcnt lgkmcnt(9)\n"
    "v_add_f32 v36, v36, v76\n"
    "v_add_f32 v37, v37, v77\n"
    "v_add_f32 v38, v38, v78\n"
    "v_add_f32 v39, v39, v79\n"
    "s_waitcnt lgkmcnt(8)\n"
    "v_add_f32 v40, v40, v80\n"
    "v_add_f32 v41, v41, v81\n"
    "v_add_f32 v42, v42, v82\n"
    "v_add_f32 v43, v43, v83\n"
    "s_waitcnt lgkmcnt(7)\n"
    "v_add_f32 v44, v44, v84\n"
    "v_add_f32 v45, v45, v85\n"
    "v_add_f32 v46, v46, v86\n"
    "v_add_f32 v47, v47, v87\n"
    "s_waitcnt lgkmcnt(6)\n"
    "v_add_f32 v48, v48, v88\n"
    "v_add_f32 v49, v49, v89\n"
    "v_add_f32 v50, v50, v90\n"
    "v_add_f32 v51, v51, v91\n"
    "s_waitcnt lgkmcnt(5)\n"
    "v_add_f32 v52, v52, v92\n"
    "v_add_f32 v53, v53, v93\n"
    "v_add_f32 v54, v54, v94\n"
    "v_add_f32 v55, v55, v95\n"
    "s_waitcnt lgkmcnt(4)\n"
    "v_add_f32 v36, v36, v96\n"
    "v_add_f32 v37, v37, v97\n"
    "v_add_f32 v38, v38, v98\n"
    "v_add_f32 v39, v39, v99\n"
    "s_waitcnt lgkmcnt(3)\n"
    "v_add_f32 v40, v40, v100\n"
    "v_add_f32 v41, v41, v101\n"
    "v_add_f32 v42, v42, v102\n"
    "v_add_f32 v43, v43, v103\n"
    "s_waitcnt lgkmcnt(2)\n"
    "v_add_f32 v44, v44, v104\n"
    "v_add_f32 v45, v45, v105\n"
    "v_add_f32 v46, v46, v106\n"
    "v_add_f32 v47, v47, v107\n"
    "s_waitcnt lgkmcnt(1)\n"
    "v_add_f32 v48, v48, v108\n"
    "v_add_f32 v49, v49, v109\n"
    "v_add_f32 v50, v50, v110\n"
    "v_add_f32 v51, v51, v111\n"
    "s_waitcnt lgkmcnt(0)\n"
    "v_add_f32 v52, v52, v112\n"
    "v_add_f32 v53, v53, v113\n"
    "v_add_f32 v54, v54, v114\n"
    "v_add_f32 v55, v55, v115\n"
    "s_mul_i32 s92, s68, 5120\n"
    "s_add_u32 s92, s92, 61440\n"
    "v_add_u32 v14, s92, v3\n"
    "ds_write_b128 v14, v[36:39] offset:0\n"
    "ds_write_b128 v14, v[40:43] offset:1024\n"
    "ds_write_b128 v14, v[44:47] offset:2048\n"
    "ds_write_b128 v14, v[48:51] offset:3072\n"
    "ds_write_b128 v14, v[52:55] offset:4096\n"
    "s_mov_b64 exec, 1\n"
    "v_mov_b32 v14, 161040\n"
    "v_mov_b32 v15, 1\n"
    "ds_add_u32 v14, v15\n"
    "s_mov_b64 exec, -1\n"
    "s_mov_b64 exec, 1\n"
    "v_mov_b32 v14, 161048\n"
    "v_mov_b32 v15, 1\n"
    "ds_add_u32 v14, v15\n"
    "s_mov_b64 exec, -1\n"
    "s_waitcnt vmcnt(9)\n"
    "v_mfma_f32_16x16x32_bf16 v[36:39], v[152:155], v[32:35], 0\n"
    "v_mfma_f32_16x16x32_bf16 v[40:43], v[156:159], v[32:35], 0\n"
    "v_mfma_f32_16x16x32_bf16 v[44:47], v[160:163], v[32:35], 0\n"
    "v_mfma_f32_16x16x32_bf16 v[48:51], v[164:167], v[32:35], 0\n"
    "v_mfma_f32_16x16x32_bf16 v[52:55], v[168:171], v[32:35], 0\n"
    "buffer_load_dwordx4 v[32:35], v7, s[24:27], s48 offen sc1\n"
    "s_waitcnt vmcnt(9)\n"
    "v_mfma_f32_16x16x32_bf16 v[56:59], v[152:155], v[116:119], 0\n"
    "v_mfma_f32_16x16x32_bf16 v[60:63], v[156:159], v[116:119], 0\n"
    "v_mfma_f32_16x16x32_bf16 v[64:67], v[160:163], v[116:119], 0\n"
    "v_mfma_f32_16x16x32_bf16 v[68:71], v[164:167], v[116:119], 0\n"
    "v_mfma_f32_16x16x32_bf16 v[72:75], v[168:171], v[116:119], 0\n"
    "buffer_load_dwordx4 v[116:119], v8, s[24:27], s48 offen sc1\n"
    "s_waitcnt vmcnt(9)\n"
    "v_mfma_f32_16x16x32_bf16 v[76:79], v[152:155], v[120:123], 0\n"
    "v_mfma_f32_16x16x32_bf16 v[80:83], v[156:159], v[120:123], 0\n"
    "v_mfma_f32_16x16x32_bf16 v[84:87], v[160:163], v[120:123], 0\n"
    "v_mfma_f32_16x16x32_bf16 v[88:91], v[164:167], v[120:123], 0\n"
    "v_mfma_f32_16x16x32_bf16 v[92:95], v[168:171], v[120:123], 0\n"
    "buffer_load_dwordx4 v[120:123], v5, s[24:27], s49 offen sc1\n"
    "s_waitcnt vmcnt(9)\n"
    "v_mfma_f32_16x16x32_bf16 v[96:99], v[152:155], v[124:127], 0\n"
    "v_mfma_f32_16x16x32_bf16 v[100:103], v[156:159], v[124:127], 0\n"
    "v_mfma_f32_16x16x32_bf16 v[104:107], v[160:163], v[124:127], 0\n"
    "v_mfma_f32_16x16x32_bf16 v[108:111], v[164:167], v[124:127], 0\n"
    "v_mfma_f32_16x16x32_bf16 v[112:115], v[168:171], v[124:127], 0\n"
    "buffer_load_dwordx4 v[152:155], v6, s[24:27], s49 offen sc1\n"
    "buffer_load_dwordx4 v[156:159], v7, s[24:27], s49 offen sc1\n"
    "buffer_load_dwordx4 v[160:163], v8, s[24:27], s49 offen sc1\n"
    "s_waitcnt vmcnt(11)\n"
    "v_mfma_f32_16x16x32_bf16 v[36:39], v[172:175], v[128:131], v[36:39]\n"
    "v_mfma_f32_16x16x32_bf16 v[40:43], v[176:179], v[128:131], v[40:43]\n"
    "v_mfma_f32_16x16x32_bf16 v[44:47], v[180:183], v[128:131], v[44:47]\n"
    "v_mfma_f32_16x16x32_bf16 v[48:51], v[184:187], v[128:131], v[48:51]\n"
    "v_mfma_f32_16x16x32_bf16 v[52:55], v[188:191], v[128:131], v[52:55]\n"
    "buffer_load_dwordx4 v[164:167], v6, s[24:27], s50 offen sc1\n"
    "s_waitcnt vmcnt(11)\n"
    "v_mfma_f32_16x16x32_bf16 v[56:59], v[172:175], v[132:135], v[56:59]\n"
    "v_mfma_f32_16x16x32_bf16 v[60:63], v[176:179], v[132:135], v[60:63]\n"
    "v_mfma_f32_16x16x32_bf16 v[64:67], v[180:183], v[132:135], v[64:67]\n"
    "v_mfma_f32_16x16x32_bf16 v[68:71], v[184:187], v[132:135], v[68:71]\n"
    "v_mfma_f32_16x16x32_bf16 v[72:75], v[188:191], v[132:135], v[72:75]\n"
    "buffer_load_dwordx4 v[168:171], v6, s[24:27], s51 offen sc1\n"
    "s_waitcnt vmcnt(11)\n"
    "v_mfma_f32_16x16x32_bf16 v[76:79], v[172:175], v[16:19], v[76:79]\n"
    "v_mfma_f32_16x16x32_bf16 v[80:83], v[176:179], v[16:19], v[80:83]\n"
    "v_mfma_f32_16x16x32_bf16 v[84:87], v[180:183], v[16:19], v[84:87]\n"
    "v_mfma_f32_16x16x32_bf16 v[88:91], v[184:187], v[16:19], v[88:91]\n"
    "v_mfma_f32_16x16x32_bf16 v[92:95], v[188:191], v[16:19], v[92:95]\n"
    "buffer_load_dwordx4 v[124:127], v7, s[24:27], s50 offen sc1\n"
    "s_waitcnt vmcnt(11)\n"
    "v_mfma_f32_16x16x32_bf16 v[96:99], v[172:175], v[20:23], v[96:99]\n"
    "v_mfma_f32_16x16x32_bf16 v[100:103], v[176:179], v[20:23], v[100:103]\n"
    "v_mfma_f32_16x16x32_bf16 v[104:107], v[180:183], v[20:23], v[104:107]\n"
    "v_mfma_f32_16x16x32_bf16 v[108:111], v[184:187], v[20:23], v[108:111]\n"
    "v_mfma_f32_16x16x32_bf16 v[112:115], v[188:191], v[20:23], v[112:115]\n"
    "buffer_load_dwordx4 v[128:131], v7, s[24:27], s51 offen sc1\n"
    "s_waitcnt vmcnt(11)\n"
    "v_mfma_f32_16x16x32_bf16 v[36:39], v[192:195], v[24:27], v[36:39]\n"
    "v_mfma_f32_16x16x32_bf16 v[40:43], v[196:199], v[24:27], v[40:43]\n"
    "v_mfma_f32_16x16x32_bf16 v[44:47], v[200:203], v[24:27], v[44:47]\n"
    "v_mfma_f32_16x16x32_bf16 v[48:51], v[204:207], v[24:27], v[48:51]\n"
    "v_mfma_f32_16x16x32_bf16 v[52:55], v[208:211], v[24:27], v[52:55]\n"
    "buffer_load_dwordx4 v[132:135], v8, s[24:27], s50 offen sc1\n"
    "s_waitcnt vmcnt(11)\n"
    "v_mfma_f32_16x16x32_bf16 v[56:59], v[192:195], v[28:31], v[56:59]\n"
    "v_mfma_f32_16x16x32_bf16 v[60:63], v[196:199], v[28:31], v[60:63]\n"
    "v_mfma_f32_16x16x32_bf16 v[64:67], v[200:203], v[28:31], v[64:67]\n"
    "v_mfma_f32_16x16x32_bf16 v[68:71], v[204:207], v[28:31], v[68:71]\n"
    "v_mfma_f32_16x16x32_bf16 v[72:75], v[208:211], v[28:31], v[72:75]\n"
    "buffer_load_dwordx4 v[16:19], v8, s[24:27], s51 offen sc1\n"
    "s_waitcnt vmcnt(11)\n"
    "v_mfma_f32_16x16x32_bf16 v[76:79], v[192:195], v[32:35], v[76:79]\n"
    "v_mfma_f32_16x16x32_bf16 v[80:83], v[196:199], v[32:35], v[80:83]\n"
    "v_mfma_f32_16x16x32_bf16 v[84:87], v[200:203], v[32:35], v[84:87]\n"
    "v_mfma_f32_16x16x32_bf16 v[88:91], v[204:207], v[32:35], v[88:91]\n"
    "v_mfma_f32_16x16x32_bf16 v[92:95], v[208:211], v[32:35], v[92:95]\n"
    "buffer_load_dwordx4 v[172:175], v5, s[24:27], s50 offen sc1\n"
    "s_waitcnt vmcnt(11)\n"
    "v_mfma_f32_16x16x32_bf16 v[96:99], v[192:195], v[116:119], v[96:99]\n"
    "v_mfma_f32_16x16x32_bf16 v[100:103], v[196:199], v[116:119], v[100:103]\n"
    "v_mfma_f32_16x16x32_bf16 v[104:107], v[200:203], v[116:119], v[104:107]\n"
    "v_mfma_f32_16x16x32_bf16 v[108:111], v[204:207], v[116:119], v[108:111]\n"
    "v_mfma_f32_16x16x32_bf16 v[112:115], v[208:211], v[116:119], v[112:115]\n"
    "buffer_load_dwordx4 v[176:179], v5, s[24:27], s51 offen sc1\n"
    "v_mov_b32 v14, 161048\n"
    "s_mov_b32 s0, 524288\n"
    "s_waitcnt lgkmcnt(0)\n"
    "Lk_lw_81:\n"
    "ds_read_b32 v15, v14\n"
    "s_waitcnt lgkmcnt(0)\n"
    "v_readfirstlane_b32 s92, v15\n"
    "s_nop 1\n"
    "s_cmp_ge_u32 s92, 4\n"
    "s_cbranch_scc1 Lk_lwd_82\n"
    "s_sub_u32 s0, s0, 1\n"
    "s_cbranch_scc0 Lk_lw_81\n"
    "Lk_lwd_82:\n"
    "s_waitcnt vmcnt(11)\n"
    "v_mfma_f32_16x16x32_bf16 v[36:39], v[212:215], v[120:123], v[36:39]\n"
    "v_mfma_f32_16x16x32_bf16 v[40:43], v[216:219], v[120:123], v[40:43]\n"
    "v_mfma_f32_16x16x32_bf16 v[44:47], v[220:223], v[120:123], v[44:47]\n"
    "v_mfma_f32_16x16x32_bf16 v[48:51], v[224:227], v[120:123], v[48:51]\n"
    "v_mfma_f32_16x16x32_bf16 v[52:55], v[228:231], v[120:123], v[52:55]\n"
    "s_waitcnt vmcnt(10)\n"
    "v_mfma_f32_16x16x32_bf16 v[56:59], v[212:215], v[152:155], v[56:59]\n"
    "v_mfma_f32_16x16x32_bf16 v[60:63], v[216:219], v[152:155], v[60:63]\n"
    "v_mfma_f32_16x16x32_bf16 v[64:67], v[220:223], v[152:155], v[64:67]\n"
    "v_mfma_f32_16x16x32_bf16 v[68:71], v[224:227], v[152:155], v[68:71]\n"
    "v_mfma_f32_16x16x32_bf16 v[72:75], v[228:231], v[152:155], v[72:75]\n"
    "s_waitcnt vmcnt(9)\n"
    "v_mfma_f32_16x16x32_bf16 v[76:79], v[212:215], v[156:159], v[76:79]\n"
    "v_mfma_f32_16x16x32_bf16 v[80:83], v[216:219], v[156:159], v[80:83]\n"
    "v_mfma_f32_16x16x32_bf16 v[84:87], v[220:223], v[156:159], v[84:87]\n"
    "v_mfma_f32_16x16x32_bf16 v[88:91], v[224:227], v[156:159], v[88:91]\n"
    "v_mfma_f32_16x16x32_bf16 v[92:95], v[228:231], v[156:159], v[92:95]\n"
    "s_waitcnt vmcnt(8)\n"
    "v_mfma_f32_16x16x32_bf16 v[96:99], v[212:215], v[160:163], v[96:99]\n"
    "v_mfma_f32_16x16x32_bf16 v[100:103], v[216:219], v[160:163], v[100:103]\n"
    "v_mfma_f32_16x16x32_bf16 v[104:107], v[220:223], v[160:163], v[104:107]\n"
    "v_mfma_f32_16x16x32_bf16 v[108:111], v[224:227], v[160:163], v[108:111]\n"
    "v_mfma_f32_16x16x32_bf16 v[112:115], v[228:231], v[160:163], v[112:115]\n"
    "s_waitcnt vmcnt(7)\n"
    "v_mfma_f32_16x16x32_bf16 v[56:59], v[232:235], v[164:167], v[56:59]\n"
    "v_mfma_f32_16x16x32_bf16 v[60:63], v[236:239], v[164:167], v[60:63]\n"
    "v_mfma_f32_16x16x32_bf16 v[64:67], v[240:243], v[164:167], v[64:67]\n"
    "v_mfma_f32_16x16x32_bf16 v[68:71], v[244:247], v[164:167], v[68:71]\n"
    "v_mfma_f32_16x16x32_bf16 v[72:75], v[248:251], v[164:167], v[72:75]\n"
    "s_waitcnt vmcnt(6)\n"
    "v_mfma_f32_16x16x32_bf16 v[56:59], v[252:255], v[168:171], v[56:59]\n"
    "v_mfma_f32_16x16x32_bf16 v[60:63], v[136:139], v[168:171], v[60:63]\n"
    "v_mfma_f32_16x16x32_bf16 v[64:67], v[140:143], v[168:171], v[64:67]\n"
    "v_mfma_f32_16x16x32_bf16 v[68:71], v[144:147], v[168:171], v[68:71]\n"
    "v_mfma_f32_16x16x32_bf16 v[72:75], v[148:151], v[168:171], v[72:75]\n"
    "s_waitcnt vmcnt(5)\n"
    "v_mfma_f32_16x16x32_bf16 v[76:79], v[232:235], v[124:127], v[76:79]\n"
    "v_mfma_f32_16x16x32_bf16 v[80:83], v[236:239], v[124:127], v[80:83]\n"
    "s_nop 0\n"
    "ds_write_b128 v11, v[56:59] offset:0\n"
    "v_mfma_f32_16x16x32_bf16 v[84:87], v[240:243], v[124:127], v[84:87]\n"
    "ds_write_b128 v11, v[60:63] offset:1024\n"
    "v_mfma_f32_16x16x32_bf16 v[88:91], v[244:247], v[124:127], v[88:91]\n"
    "ds_write_b128 v11, v[64:67] offset:2048\n"
    "v_mfma_f32_16x16x32_bf16 v[92:95], v[248:251], v[124:127], v[92:95]\n"
    "ds_write_b128 v11, v[68:71] offset:3072\n"
    "s_waitcnt vmcnt(4)\n"
    "v_mfma_f32_16x16x32_bf16 v[76:79], v[252:255], v[128:131], v[76:79]\n"
    "v_mfma_f32_16x16x32_bf16 v[80:83], v[136:139], v[128:131], v[80:83]\n"
    "v_mfma_f32_16x16x32_bf16 v[84:87], v[140:143], v[128:131], v[84:87]\n"
    "v_mfma_f32_16x16x32_bf16 v[88:91], v[144:147], v[128:131], v[88:91]\n"
    "v_mfma_f32_16x16x32_bf16 v[92:95], v[148:151], v[128:131], v[92:95]\n"
    "ds_write_b128 v11, v[72:75] offset:4096\n"
    "s_waitcnt vmcnt(3)\n"
    "v_mfma_f32_16x16x32_bf16 v[96:99], v[232:235], v[132:135], v[96:99]\n"
    "v_mfma_f32_16x16x32_bf16 v[100:103], v[236:239], v[132:135], v[100:103]\n"
    "ds_write_b128 v12, v[76:79] offset:0\n"
    "v_mfma_f32_16x16x32_bf16 v[104:107], v[240:243], v[132:135], v[104:107]\n"
    "ds_write_b128 v12, v[80:83] offset:1024\n"
    "v_mfma_f32_16x16x32_bf16 v[108:111], v[244:247], v[132:135], v[108:111]\n"
    "ds_write_b128 v12, v[84:87] offset:2048\n"
    "v_mfma_f32_16x16x32_bf16 v[112:115], v[248:251], v[132:135], v[112:115]\n"
    "ds_write_b128 v12, v[88:91] offset:3072\n"
    "s_waitcnt vmcnt(2)\n"
    "v_mfma_f32_16x16x32_bf16 v[96:99], v[252:255], v[16:19], v[96:99]\n"
    "v_mfma_f32_16x16x32_bf16 v[100:103], v[136:139], v[16:19], v[100:103]\n"
    "v_mfma_f32_16x16x32_bf16 v[104:107], v[140:143], v[16:19], v[104:107]\n"
    "v_mfma_f32_16x16x32_bf16 v[108:111], v[144:147], v[16:19], v[108:111]\n"
    "v_mfma_f32_16x16x32_bf16 v[112:115], v[148:151], v[16:19], v[112:115]\n"
    "ds_write_b128 v12, v[92:95] offset:4096\n"
    "s_waitcnt vmcnt(1)\n"
    "v_mfma_f32_16x16x32_bf16 v[36:39], v[232:235], v[172:175], v[36:39]\n"
    "s_nop 0\n"
    "ds_write_b128 v13, v[96:99] offset:0\n"
    "v_mfma_f32_16x16x32_bf16 v[40:43], v[236:239], v[172:175], v[40:43]\n"
    "ds_write_b128 v13, v[100:103] offset:1024\n"
    "v_mfma_f32_16x16x32_bf16 v[44:47], v[240:243], v[172:175], v[44:47]\n"
    "ds_write_b128 v13, v[104:107] offset:2048\n"
    "v_mfma_f32_16x16x32_bf16 v[48:51], v[244:247], v[172:175], v[48:51]\n"
    "ds_write_b128 v13, v[108:111] offset:3072\n"
    "v_mfma_f32_16x16x32_bf16 v[52:55], v[248:251], v[172:175], v[52:55]\n"
    "ds_write_b128 v13, v[112:115] offset:4096\n"
    "s_waitcnt vmcnt(0)\n"
    "v_mfma_f32_16x16x32_bf16 v[36:39], v[252:255], v[176:179], v[36:39]\n"
    "v_mfma_f32_16x16x32_bf16 v[40:43], v[136:139], v[176:179], v[40:43]\n"
    "v_mfma_f32_16x16x32_bf16 v[44:47], v[140:143], v[176:179], v[44:47]\n"
    "v_mfma_f32_16x16x32_bf16 v[48:51], v[144:147], v[176:179], v[48:51]\n"
    "v_mfma_f32_16x16x32_bf16 v[52:55], v[148:151], v[176:179], v[52:55]\n"
    "s_mov_b64 exec, 1\n"
    "v_mov_b32 v14, 161052\n"
    "v_mov_b32 v15, 1\n"
    "ds_add_u32 v14, v15\n"
    "s_mov_b64 exec, -1\n"
    "v_mov_b32 v14, 161052\n"
    "s_mov_b32 s0, 524288\n"
    "s_waitcnt lgkmcnt(0)\n"
    "Lk_lw_83:\n"
    "ds_read_b32 v15, v14\n"
    "s_waitcnt lgkmcnt(0)\n"
    "v_readfirstlane_b32 s92, v15\n"
    "s_nop 1\n"
    "s_cmp_ge_u32 s92, 4\n"
    "s_cbranch_scc1 Lk_lwd_84\n"
    "s_sub_u32 s0, s0, 1\n"
    "s_cbranch_scc0 Lk_lw_83\n"
    "Lk_lwd_84:\n"
    "s_mul_i32 s92, s68, 15360\n"
    "s_add_u32 s92, s92, 0\n"
    "v_add_u32 v14, s92, v3\n"
    "ds_read_b128 v[56:59], v14 offset:0\n"
    "ds_read_b128 v[60:63], v14 offset:1024\n"
    "ds_read_b128 v[64:67], v14 offset:2048\n"
    "ds_read_b128 v[68:71], v14 offset:3072\n"
    "ds_read_b128 v[72:75], v14 offset:4096\n"
    "ds_read_b128 v[76:79], v14 offset:5120\n"
    "ds_read_b128 v[80:83], v14 offset:6144\n"
    "ds_read_b128 v[84:87], v14 offset:7168\n"
    "ds_read_b128 v[88:91], v14 offset:8192\n"
    "ds_read_b128 v[92:95], v14 offset:9216\n"
    "ds_read_b128 v[96:99], v14 offset:10240\n"
    "ds_read_b128 v[100:103], v14 offset:11264\n"
    "ds_read_b128 v[104:107], v14 offset:12288\n"
    "ds_read_b128 v[108:111], v14 offset:13312\n"
    "ds_read_b128 v[112:115], v14 offset:14336\n"
    "s_waitcnt lgkmcnt(14)\n"
    "v_add_f32 v36, v36, v56\n"
    "v_add_f32 v37, v37, v57\n"
    "v_add_f32 v38, v38, v58\n"
    "v_add_f32 v39, v39, v59\n"
    "s_waitcnt lgkmcnt(13)\n"
    "v_add_f32 v40, v40, v60\n"
    "v_add_f32 v41, v41, v61\n"
    "v_add_f32 v42, v42, v62\n"
    "v_add_f32 v43, v43, v63\n"
    "s_waitcnt lgkmcnt(12)\n"
    "v_add_f32 v44, v44, v64\n"
    "v_add_f32 v45, v45, v65\n"
    "v_add_f32 v46, v46, v66\n"
    "v_add_f32 v47, v47, v67\n"
    "s_waitcnt lgkmcnt(11)\n"
    "v_add_f32 v48, v48, v68\n"
    "v_add_f32 v49, v49, v69\n"
    "v_add_f32 v50, v50, v70\n"
    "v_add_f32 v51, v51, v71\n"
    "s_waitcnt lgkmcnt(10)\n"
    "v_add_f32 v52, v52, v72\n"
    "v_add_f32 v53, v53, v73\n"
    "v_add_f32 v54, v54, v74\n"
    "v_add_f32 v55, v55, v75\n"
    "s_waitcnt lgkmcnt(9)\n"
    "v_add_f32 v36, v36, v76\n"
    "v_add_f32 v37, v37, v77\n"
    "v_add_f32 v38, v38, v78\n"
    "v_add_f32 v39, v39, v79\n"
    "s_waitcnt lgkmcnt(8)\n"
    "v_add_f32 v40, v40, v80\n"
    "v_add_f32 v41, v41, v81\n"
    "v_add_f32 v42, v42, v82\n"
    "v_add_f32 v43, v43, v83\n"
    "s_waitcnt lgkmcnt(7)\n"
    "v_add_f32 v44, v44, v84\n"
    "v_add_f32 v45, v45, v85\n"
    "v_add_f32 v46, v46, v86\n"
    "v_add_f32 v47, v47, v87\n"
    "s_waitcnt lgkmcnt(6)\n"
    "v_add_f32 v48, v48, v88\n"
    "v_add_f32 v49, v49, v89\n"
    "v_add_f32 v50, v50, v90\n"
    "v_add_f32 v51, v51, v91\n"
    "s_waitcnt lgkmcnt(5)\n"
    "v_add_f32 v52, v52, v92\n"
    "v_add_f32 v53, v53, v93\n"
    "v_add_f32 v54, v54, v94\n"
    "v_add_f32 v55, v55, v95\n"
    "s_waitcnt lgkmcnt(4)\n"
    "v_add_f32 v36, v36, v96\n"
    "v_add_f32 v37, v37, v97\n"
    "v_add_f32 v38, v38, v98\n"
    "v_add_f32 v39, v39, v99\n"
    "s_waitcnt lgkmcnt(3)\n"
    "v_add_f32 v40, v40, v100\n"
    "v_add_f32 v41, v41, v101\n"
    "v_add_f32 v42, v42, v102\n"
    "v_add_f32 v43, v43, v103\n"
    "s_waitcnt lgkmcnt(2)\n"
    "v_add_f32 v44, v44, v104\n"
    "v_add_f32 v45, v45, v105\n"
    "v_add_f32 v46, v46, v106\n"
    "v_add_f32 v47, v47, v107\n"
    "s_waitcnt lgkmcnt(1)\n"
    "v_add_f32 v48, v48, v108\n"
    "v_add_f32 v49, v49, v109\n"
    "v_add_f32 v50, v50, v110\n"
    "v_add_f32 v51, v51, v111\n"
    "s_waitcnt lgkmcnt(0)\n"
    "v_add_f32 v52, v52, v112\n"
    "v_add_f32 v53, v53, v113\n"
    "v_add_f32 v54, v54, v114\n"
    "v_add_f32 v55, v55, v115\n"
    "v_mov_b32 v14, 161044\n"
    "s_mov_b32 s0, 524288\n"
    "Lk_lw_85:\n"
    "ds_read_b32 v15, v14\n"
    "s_waitcnt lgkmcnt(0)\n"
    "v_readfirstlane_b32 s92, v15\n"
    "s_nop 1\n"
    "s_cmp_ge_u32 s92, 4\n"
    "s_cbranch_scc1 Lk_lwd_86\n"
    "s_sub_u32 s0, s0, 1\n"
    "s_cbranch_scc0 Lk_lw_85\n"
    "Lk_lwd_86:\n"
    "s_mul_i32 s92, s68, 5120\n"
    "s_add_u32 s92, s92, 61440\n"
    "v_add_u32 v14, s92, v3\n"
    "ds_write_b128 v14, v[36:39] offset:0\n"
    "ds_write_b128 v14, v[40:43] offset:1024\n"
    "ds_write_b128 v14, v[44:47] offset:2048\n"
    "ds_write_b128 v14, v[48:51] offset:3072\n"
    "ds_write_b128 v14, v[52:55] offset:4096\n"
    "s_waitcnt lgkmcnt(0)\n"
    "s_barrier\n"
    "s_lshl_b32 s92, s6, 2\n"
    "s_add_u32 s92, s92, 3\n"
    "s_cmp_eq_u32 s92, s68\n"
    "s_cbranch_scc0 Lk_skip_87\n"
    "v_lshlrev_b32 v11, 6, v0\n"
    "s_mov_b32 s40, 8192\n"
    "s_mov_b32 s41, 12288\n"
    "s_mov_b32 s42, 16384\n"
    "s_mov_b32 s43, 20480\n"
    "s_mov_b32 s0, 131072\n"
    "s_nop 4\n"
    "Lk_poll_88:\n"
    "buffer_load_dword v16, v11, s[24:27], s40 offen sc1\n"
    "buffer_load_dword v17, v11, s[24:27], s41 offen sc1\n"
    "buffer_load_dword v18, v11, s[24:27], s42 offen sc1\n"
    "buffer_load_dword v19, v11, s[24:27], s43 offen sc1\n"
    "Lk_chk_90:\n"
    "s_waitcnt vmcnt(0)\n"
    "v_xor_b32 v16, s73, v16\n"
    "v_xor_b32 v17, s73, v17\n"
    "v_xor_b32 v18, s73, v18\n"
    "v_xor_b32 v19, s73, v19\n"
    "v_or_b32 v16, v16, v17\n"
    "v_or3_b32 v16, v16, v18, v19\n"
    "v_cmp_ne_u32 vcc, 0, v16\n"
    "s_nop 4\n"
    "s_cbranch_vccz Lk_done_89\n"
    "s_sleep 4\n"
    "s_sub_u32 s0, s0, 1\n"
    "s_cbranch_scc0 Lk_poll_88\n"
    "Lk_done_89:\n"
    "s_mov_b32 s94, 0\n"
    "s_mov_b64 exec, 1\n"
    "v_mov_b32 v14, s73\n"
    "v_mov_b32 v15, 0\n"
    "buffer_store_dword v14, v15, s[24:27], s94 offen sc1\n"
    "s_mov_b64 exec, -1\n"
    "Lk_skip_87:\n"
    "s_endpgm\n"
    "Lk_end:\n"

    :: "s"(kp), "s"(bid), "{v0}"(tid), "s"(lds0)
    : "memory", "vcc", "s4", "s5", "s6", "s7", "s8", "s9", "s10", "s11", "s12", "s13", "s14", "s15", "s16", "s17", "s18", "s19", "s20", "s21", "s22", "s23", "s24", "s25", "s26", "s27", "s28", "s29", "s30", "s31", "s33", "s34", "s35", "s36", "s37", "s38", "s39", "s40", "s41", "s42", "s43", "s44", "s45", "s46", "s47", "s48", "s49", "s50", "s51", "s52", "s53", "s54", "s55", "s56", "s57", "s58", "s59", "s60", "s61", "s62", "s63", "s64", "s65", "s66", "s67", "s68", "s69", "s70", "s71", "s72", "s73", "s74", "s75", "s76", "s77", "s78", "s79", "s80", "s81", "s82", "s83", "s84", "s85", "s86", "s87", "s88", "s89", "s90", "s91", "s92", "s93", "s94", "s95", "s96", "s97", "s98", "s99", "v1", "v2", "v3", "v4", "v5", "v6", "v7", "v8", "v9", "v10", "v11", "v12", "v13", "v14", "v15", "v16", "v17", "v18", "v19", "v20", "v21", "v22", "v23", "v24", "v25", "v26", "v27", "v28", "v29", "v30", "v31", "v32", "v33", "v34", "v35", "v36", "v37", "v38", "v39", "v40", "v41", "v42", "v43", "v44", "v45", "v46", "v47", "v48", "v49", "v50", "v51", "v52", "v53", "v54", "v55", "v56", "v57", "v58", "v59", "v60", "v61", "v62", "v63", "v64", "v65", "v66", "v67", "v68", "v69", "v70", "v71", "v72", "v73", "v74", "v75", "v76", "v77", "v78", "v79", "v80", "v81", "v82", "v83", "v84", "v85", "v86", "v87", "v88", "v89", "v90", "v91", "v92", "v93", "v94", "v95", "v96", "v97", "v98", "v99", "v100", "v101", "v102", "v103", "v104", "v105", "v106", "v107", "v108", "v109", "v110", "v111", "v112", "v113", "v114", "v115", "v116", "v117", "v118", "v119", "v120", "v121", "v122", "v123", "v124", "v125", "v126", "v127", "v128", "v129", "v130", "v131", "v132", "v133", "v134", "v135", "v136", "v137", "v138", "v139", "v140", "v141", "v142", "v143", "v144", "v145", "v146", "v147", "v148", "v149", "v150", "v151", "v152", "v153", "v154", "v155", "v156", "v157", "v158", "v159", "v160", "v161", "v162", "v163", "v164", "v165", "v166", "v167", "v168", "v169", "v170", "v171", "v172", "v173", "v174", "v175", "v176", "v177", "v178", "v179", "v180", "v181", "v182", "v183", "v184", "v185", "v186", "v187", "v188", "v189", "v190", "v191", "v192", "v193", "v194", "v195", "v196", "v197", "v198", "v199", "v200", "v201", "v202", "v203", "v204", "v205", "v206", "v207", "v208", "v209", "v210", "v211", "v212", "v213", "v214", "v215", "v216", "v217", "v218", "v219", "v220", "v221", "v222", "v223", "v224", "v225", "v226", "v227", "v228", "v229", "v230", "v231", "v232", "v233", "v234", "v235", "v236", "v237", "v238", "v239", "v240", "v241", "v242", "v243", "v244", "v245", "v246", "v247", "v248", "v249", "v250", "v251", "v252", "v253", "v254", "v255");
#endif
}

extern "C" void kernel_launch(void* const* d_in, const int* in_sizes, int n_in, void* d_out, int out_size,
                              void* d_ws, size_t ws_size, hipStream_t stream) {
  const int idx[15] = {0, 3, 4, 10, -1, 12, 5, 6, 1, 2, 7, 11, 8, 13, -2};
  Args a;
  for (int i = 0; i < 15; i++) {
    if (idx[i] >= 0) { a.d[i].p = d_in[idx[i]]; a.d[i].bytes = (uint32_t)in_sizes[idx[i]] * 2u; }
    else if (idx[i] == -1) { a.d[i].p = d_ws; a.d[i].bytes = 20971520u; }
    else { a.d[i].p = d_out; a.d[i].bytes = (uint32_t)out_size * 4u; }
    a.d[i].fmt = 0x00020000u;
  }
  s5_mlp_fused<<<256, 512, 0, stream>>>(a);
}
```
